```python
import torch

def _softmax(x, axis=-1):
    return torch.softmax(x, dim=axis)
def _gelu(x):
    return torch.nn.functional.gelu(x, approximate="tanh")
def _silu(x):
    return torch.nn.functional.silu(x)
def _sigmoid(x):
    return torch.sigmoid(x)
def _layernorm(x, axis=-1, eps=1e-5):
    m = x.mean(dim=axis, keepdim=True)
    v = x.var(dim=axis, keepdim=True, unbiased=False)
    return (x - m) / torch.sqrt(v + eps)
def _scatter_add(tgt, idx, src, axis=0):
    return tgt.clone().index_add_(axis, idx, src)
def _segment_sum(src, seg, n):
    out = torch.zeros((n,) + src.shape[1:], dtype=src.dtype)
    return out.index_add_(0, seg, src)
def _pad(x, pw):
    flat = tuple(v for pair in reversed(pw) for v in pair)
    return torch.nn.functional.pad(x, flat)

kernel_name = 'gated_mix__grouped_kv_mqa__ssm_conv_s4d__prompt'

def setup_inputs():

    _w = (lambda a: (lambda t: t.to(torch.bfloat16) if t.is_floating_point() else t)(torch.as_tensor(a)))
    def _A_setup():
        B, S, D, H = 1, 2048, 512, 32
        Dh = D // H
        return {
            "x": _w((lambda *s: torch.randn(*s, dtype=torch.bfloat16) * 0.05)(B, S, D)),
            "wq": _w((lambda *s: torch.randn(*s, dtype=torch.bfloat16) * 0.8)(D, H * Dh)),
            "wk": _w((lambda *s: torch.randn(*s, dtype=torch.bfloat16) * 0.8)(D, Dh)),
            "wv": _w((lambda *s: torch.randn(*s, dtype=torch.bfloat16) * 0.05)(D, Dh)),
            "wo": _w((lambda *s: torch.randn(*s, dtype=torch.bfloat16) * 0.05)(H * Dh, D)),
        }
    _A_in = _A_setup()

    def _B_setup():
        Bsz, L, D, N = 1, 2048, 512, 128
        return {
            "u": _w((lambda *s: torch.randn(*s, dtype=torch.bfloat16) * 0.05)(Bsz, L, D)),
            "log_A_real": _w((lambda *s: torch.randn(*s, dtype=torch.bfloat16) * 6.4)(D, N) * 0.5),
            "A_imag": _w((lambda *s: torch.randn(*s, dtype=torch.bfloat16) * 0.05)(D, N) * 3.14),
            "C_real": _w((lambda *s: torch.randn(*s, dtype=torch.bfloat16) * 0.05)(D, N) / N ** 0.5),
            "C_imag": _w((lambda *s: torch.randn(*s, dtype=torch.bfloat16) * 0.05)(D, N) / N ** 0.5),
            "log_dt": _w((lambda *s: torch.randn(*s, dtype=torch.bfloat16) * 0.05)(D) * 0.5 - 4.0),
            "D_skip": _w((lambda *s: torch.randn(*s, dtype=torch.bfloat16) * 0.05)(D)),
        }
    _B_in = _B_setup()

    return {
        "A_x": _A_in["x"],
        "A_wq": _A_in["wq"],
        "A_wk": _A_in["wk"],
        "A_wv": _A_in["wv"],
        "A_wo": _A_in["wo"],
        "B_u": _B_in["u"],
        "B_log_A_real": _B_in["log_A_real"],
        "B_A_imag": _B_in["A_imag"],
        "B_C_real": _B_in["C_real"],
        "B_C_imag": _B_in["C_imag"],
        "B_log_dt": _B_in["log_dt"],
        "B_D_skip": _B_in["D_skip"],
        "gate_w": _w((lambda *s: torch.randn(*s, dtype=torch.bfloat16) * 0.05)(512) * 16.0),
    }

def reference(A_x, A_wq, A_wk, A_wv, A_wo, B_u, B_log_A_real, B_A_imag, B_C_real, B_C_imag, B_log_dt, B_D_skip, gate_w):
    def _A_ref(x, wq, wk, wv, wo):
        B, S, D = x.shape
        H = 32
        Dh = D // H
        q = (lambda x, *d: x.permute(*d))((x @ wq).reshape(B, S, H, Dh), 0, 2, 1, 3)
        k = x @ wk
        v = x @ wv
        scores = torch.einsum("bhqd,bkd->bhqk", q, k) * (Dh ** -0.5)
        p = _softmax(scores, axis=-1)
        o = torch.einsum("bhqk,bkd->bhqd", p, v)
        return (lambda x, *d: x.permute(*d))(o, 0, 2, 1, 3).reshape(B, S, H * Dh) @ wo

    def _B_ref(u, log_A_real, A_imag, C_real, C_imag, log_dt, D_skip):
        L = u.shape[1]
        dt = torch.exp(log_dt)
        A = -torch.exp(log_A_real) + 1j * A_imag
        C = (C_real + 1j * C_imag) * dt[:, None]
        pos = (lambda x, dt: x.to(dt))(torch.arange(L), log_dt.dtype)
        P = torch.exp((A * dt[:, None])[:, :, None] * pos[None, None, :])
        K = 2.0 * torch.einsum("dn,dnl->dl", C, P).real
        L2 = 2 * L
        u_t = (lambda x, *d: x.permute(*d))(u, 0, 2, 1)
        Kf = (lambda x, *a, **k: torch.fft.rfft(x.float(), *a, **k))(K, L2)
        Uf = (lambda x, *a, **k: torch.fft.rfft(x.float(), *a, **k))(u_t, L2)
        y = (lambda x, *a, **k: torch.fft.irfft(x, *a, **k).to(torch.bfloat16))(Uf * Kf[None, :, :], L2)[:, :, :L]
        y = (lambda x, *d: x.permute(*d))(y, 0, 2, 1)
        return y + u * D_skip[None, None, :]

    x = A_x
    g = _sigmoid((x * gate_w).sum(-1, keepdims=True))
    a = _A_ref(x, A_wq, A_wk, A_wv, A_wo)
    b = _B_ref(x, B_log_A_real, B_A_imag, B_C_real, B_C_imag, B_log_dt, B_D_skip)
    return g * a + (1 - g) * b
```

```cpp
#include <hip/hip_runtime.h>
#include <stdint.h>

typedef unsigned short u16;
typedef unsigned int u32;
typedef unsigned long long u64;
typedef __bf16 bf16x2 __attribute__((ext_vector_type(2)));
typedef __bf16 bf16x8 __attribute__((ext_vector_type(8)));
typedef __bf16 bf16x16 __attribute__((ext_vector_type(16)));
typedef _Float16 f16x2 __attribute__((ext_vector_type(2)));
typedef _Float16 f16x8 __attribute__((ext_vector_type(8)));
typedef float f32x2 __attribute__((ext_vector_type(2)));
typedef float f32x4 __attribute__((ext_vector_type(4)));
typedef float f32x8 __attribute__((ext_vector_type(8)));
typedef float f32x16 __attribute__((ext_vector_type(16)));
typedef u32 u32x2 __attribute__((ext_vector_type(2)));
typedef u32 u32x4 __attribute__((ext_vector_type(4)));
typedef u32 u32x8 __attribute__((ext_vector_type(8)));
typedef short s16x4 __attribute__((ext_vector_type(4)));
#define LAS __attribute__((address_space(3)))
#define GAS __attribute__((address_space(1)))
#define DI __device__ __forceinline__
#define RLX_AGENT __ATOMIC_RELAXED, __HIP_MEMORY_SCOPE_AGENT
typedef LAS char* lptr;

constexpr int SEQ = 2048, DM = 512, NMODE = 128;
constexpr size_t WS_QF = 0;
constexpr size_t WS_KF = WS_QF + 2097152;
constexpr size_t WS_VF = WS_KF + 65536;
constexpr size_t WS_XT = WS_VF + 65536;
constexpr size_t WS_YT = WS_XT + 2097152;
constexpr size_t WS_WOF = WS_YT + 2097152;
constexpr size_t WS_KAP = WS_WOF + 524288;
constexpr size_t WS_FLAG = WS_KAP + 2097152;

struct Params {
  const u16 *x, *wq, *wk, *wv, *wo, *logA, *Aim, *Cre, *Cim, *logdt, *Dskip, *gatew;
  u16* out;
  char* ws;
};

DI float bf2f(u16 v) { return __uint_as_float((u32)v << 16); }
DI float bflo(u32 v) { return __uint_as_float(v << 16); }
DI float bfhi(u32 v) { return __uint_as_float(v & 0xffff0000u); }
DI u32 pkbf(float a, float b) { f32x2 v = {a, b}; return __builtin_bit_cast(u32, __builtin_convertvector(v, bf16x2)); }
DI float bfr(float a) { return bflo(pkbf(a, a)); }
DI u32 pkh(float a, float b) { f32x2 v = {a, b}; return __builtin_bit_cast(u32, __builtin_convertvector(v, f16x2)); }
DI f32x4 mfma16(bf16x8 a, bf16x8 b, f32x4 c) { return __builtin_amdgcn_mfma_f32_16x16x32_bf16(a, b, c, 0, 0, 0); }
DI f32x16 mfma32(bf16x8 a, bf16x8 b, f32x16 c) { return __builtin_amdgcn_mfma_f32_32x32x16_bf16(a, b, c, 0, 0, 0); }
template <class T> DI T lds_ld(lptr p) { return *(LAS T*)p; }
template <class T> DI void lds_st(lptr p, T v) { *(LAS T*)p = v; }
template <class T> DI T gld(const void* p) { return *(const GAS T*)p; }
template <class T> DI void gst(void* p, T v) { *(GAS T*)p = v; }
typedef __amdgpu_buffer_rsrc_t rsrc_t;
constexpr u32 WS_BYTES = (u32)WS_FLAG + 1024;
DI rsrc_t ws_rsrc(char* ws) { return __builtin_amdgcn_make_buffer_rsrc(ws, 0, WS_BYTES, 0x00020000); }
DI u32x4 bld4(rsrc_t r, u32 voff, u32 soff) { return __builtin_amdgcn_raw_buffer_load_b128(r, voff, soff, 0); }
DI bf16x8 bld8(rsrc_t r, u32 voff, u32 soff) { return __builtin_bit_cast(bf16x8, bld4(r, voff, soff)); }
DI bf16x8 trfrag(lptr p, int hi_off) {
  s16x4 lo = __builtin_amdgcn_ds_read_tr16_b64_v4i16((LAS s16x4*)p);
  s16x4 hi = __builtin_amdgcn_ds_read_tr16_b64_v4i16((LAS s16x4*)(p + hi_off));
  return __builtin_bit_cast(bf16x8, __builtin_shufflevector(lo, hi, 0, 1, 2, 3, 4, 5, 6, 7));
}

constexpr int LA_WQ = 0, LA_WKV = 65536, LA_XS = 81920, LA_XY = 90112, XROW = 528, XYCH = 63 * XROW;
constexpr int LA_BYTES = LA_XY + 2 * XYCH;

DI void phaseA_proj(const Params& p, lptr lds, int m, int w, int lane) {
  const int i = m >> 3, j = m & 7, T0 = 64 * i;
  const int tq = w & 3, nh = w >> 2, l16 = lane & 15, g = lane >> 4;
  const int t512 = w * 64 + lane;
  const int srow = t512 >> 3, sc = t512 & 7;
  const int ssw = (((srow >> 1) & 1) | (((srow >> 3) & 1) << 1)) << 1;
  const u16* wkv = (j & 1) ? p.wv : p.wk;
  u32x4 rkv[2], rxs, rwq[8];
#pragma unroll
  for (int it = 0; it < 8; ++it) rwq[it] = gld<u32x4>(p.wq + (size_t)(srow + 64 * it) * DM + 64 * j + 8 * sc);
#pragma unroll
  for (int it = 0; it < 2; ++it) rkv[it] = gld<u32x4>(wkv + (size_t)(t512 + 512 * it) * 8);
  rxs = gld<u32x4>(p.x + (size_t)(T0 + srow) * DM + 64 * j + 8 * sc);
  bf16x8 xf[16];
  const u16* xrow = p.x + (size_t)(T0 + 16 * tq + l16) * DM + 8 * g;
#pragma unroll
  for (int s = 0; s < 16; ++s) xf[s] = gld<bf16x8>(xrow + 32 * s);
  const int soff = srow * 128 + ((sc ^ ssw) << 4);
#pragma unroll
  for (int it = 0; it < 8; ++it) lds_st<u32x4>(lds + LA_WQ + soff + it * 8192, rwq[it]);
#pragma unroll
  for (int it = 0; it < 2; ++it) lds_st<u32x4>(lds + LA_WKV + (t512 + 512 * it) * 16, rkv[it]);
  lds_st<u32x4>(lds + LA_XS + soff, rxs);
  __syncthreads();
  const int q4 = l16 >> 2, pp = l16 & 3;
  const int rsw = (((q4 >> 1) & 1) | ((g & 1) << 1)) << 1;
  const int trow = (8 * g + q4) * 128 + 8 * (pp & 1);
  {
    const int ct = w & 3, th = w >> 2;
    bf16x8 v = trfrag(lds + LA_XS + th * 4096 + trow + (((2 * ct + (pp >> 1)) ^ rsw) << 4), 512);
    gst<bf16x8>(p.ws + WS_XT + ((size_t)(64 * j + 16 * ct + l16) * SEQ + T0 + 32 * th + 8 * g) * 2, v);
  }
  if (w == (j >> 1)) {
    f32x4 acc = {0.f, 0.f, 0.f, 0.f};
    lptr b = lds + LA_WKV + (8 * g + q4) * 32 + 8 * pp;
    const int tb = T0 + 16 * tq;
    if (j & 1) {
#pragma unroll
      for (int s = 0; s < 16; ++s) acc = mfma16(xf[s], trfrag(b + s * 1024, 128), acc);
      u32x2 o = {pkbf(acc[0], acc[1]), pkbf(acc[2], acc[3])};
      gst<u32x2>(p.ws + WS_VF + (size_t)(tb >> 4) * 512 + ((g >> 1) * 16 + l16) * 16 + 8 * (g & 1), o);
    } else {
#pragma unroll
      for (int s = 0; s < 16; ++s) acc = mfma16(trfrag(b + s * 1024, 128), xf[s], acc);
      u32x2 o = {pkbf(acc[0], acc[1]), pkbf(acc[2], acc[3])};
      const int t = tb + l16;
      gst<u32x2>(p.ws + WS_KF + (size_t)(t >> 5) * 1024 + ((t & 31) + 32 * (g >> 1)) * 16 + 8 * (g & 1), o);
    }
  }
  {
    f32x4 a0 = {0.f, 0.f, 0.f, 0.f}, a1 = a0;
    lptr b0 = lds + LA_WQ + trow + (((4 * nh + (pp >> 1)) ^ rsw) << 4);
    lptr b1 = lds + LA_WQ + trow + (((4 * nh + 2 + (pp >> 1)) ^ rsw) << 4);
#pragma unroll
    for (int s = 0; s < 16; ++s) {
      a0 = mfma16(trfrag(b0 + s * 4096, 512), xf[s], a0);
      a1 = mfma16(trfrag(b1 + s * 4096, 512), xf[s], a1);
    }
    const float c = 0.25f * 1.4426950408889634f;
    const int t = T0 + 16 * tq + l16;
    char* dst = p.ws + WS_QF + ((size_t)((t >> 3) * 8 + j) * 64 + (t & 7) + 32 * (g >> 1)) * 16 + 8 * (g & 1);
    u32x2 o0 = {pkbf(a0[0] * c, a0[1] * c), pkbf(a0[2] * c, a0[3] * c)};
    u32x2 o1 = {pkbf(a1[0] * c, a1[1] * c), pkbf(a1[2] * c, a1[3] * c)};
    gst<u32x2>(dst + (2 * nh) * 128, o0);
    gst<u32x2>(dst + (2 * nh + 1) * 128, o1);
  }
}

DI f32x2 cmul(f32x2 a, f32x2 b) {
  f32x2 r;
  r[0] = __builtin_fmaf(a[0], b[0], -(a[1] * b[1]));
  r[1] = __builtin_fmaf(a[0], b[1], a[1] * b[0]);
  return r;
}
DI f32x2 cexp_t(float lr, float fr, float t) {
  const float mag = __builtin_amdgcn_exp2f(lr * t);
  const float ph = __builtin_amdgcn_fractf(fr * t);
  f32x2 r = {mag * __builtin_amdgcn_cosf(ph), mag * __builtin_amdgcn_sinf(ph)};
  return r;
}
DI float wave_max(float v) {
#pragma unroll
  for (int o = 32; o >= 1; o >>= 1) v = fmaxf(v, __shfl_xor(v, o));
  return v;
}

DI void phaseA_svc(const Params& p, lptr lds, int m, int sw, int lane, u32x2 (&kout)[4]) {
  const int ch = 2 * m + (sw >> 1), n = 64 * (sw & 1) + lane;
  const int l16 = lane & 15, g = lane >> 4;
  const float lar = bf2f(gld<u16>(p.logA + ch * NMODE + n));
  const float aim = bf2f(gld<u16>(p.Aim + ch * NMODE + n));
  const float cr = bf2f(gld<u16>(p.Cre + ch * NMODE + n));
  const float ci = bf2f(gld<u16>(p.Cim + ch * NMODE + n));
  const float cr2 = bf2f(gld<u16>(p.Cre + ch * NMODE + (n ^ 64)));
  const float ci2 = bf2f(gld<u16>(p.Cim + ch * NMODE + (n ^ 64)));
  const float ldt = bf2f(gld<u16>(p.logdt + ch));
  if (sw == 3 && (lane >> 5) == (m & 1)) {
    const int u = m >> 1, hf = u & 1, kb = (u >> 1) & 1, s2 = (u >> 2) & 3, r = u >> 4;
    const u16* src = p.wo + (size_t)(64 * r + 32 * kb + 16 * hf + 4 * g) * DM + 128 * s2 + 8 * l16;
    u32x4 v[4];
#pragma unroll
    for (int e = 0; e < 4; ++e) v[e] = gld<u32x4>(src + e * DM);
    char* dst = p.ws + WS_WOF + (size_t)(((r * 4 + s2) * 2 + kb) * 8 + hf) * 1024 + lane * 16;
#pragma unroll
    for (int nt = 0; nt < 4; ++nt) { u32x4 o = {v[0][nt], v[1][nt], v[2][nt], v[3][nt]}; gst<u32x4>(dst + nt * 2048, o); }
  }
  const float dt = bfr(expf(ldt));
  const float zr = -bfr(expf(lar)) * dt, zi = aim * dt;
  const float lr = zr * 1.4426950408889634f, fr = zi * 0.15915494309189535f;
  float s = wave_max(fmaxf(fmaxf(fabsf(cr), fabsf(ci)), fmaxf(fabsf(cr2), fabsf(ci2))));
  s = fmaxf(s, 1e-30f);
  const float inv = 16384.f / s;
  const f32x2 c0 = {cr * inv, ci * inv};
  const f32x2 e1 = cexp_t(lr, fr, 1.f), e16 = cexp_t(lr, fr, 16.f), e256 = cexp_t(lr, fr, 256.f);
  const f32x2 e2 = cmul(e1, e1), e4 = cmul(e2, e2), e8 = cmul(e4, e4);
  lptr img = lds + LA_XY + (sw >> 1) * XYCH;
  lptr wp = img + n * 4;
#define PUTX(row, v) lds_st<u32>(wp + (row) * XROW, pkh((v)[0], -(v)[1]))
  {
    f32x2 e = c0, y = {16384.f, 0.f};
#pragma unroll
    for (int r = 0; r < 16; ++r) {
      PUTX(r, e);
      lds_st<u32>(wp + (47 + r) * XROW, pkh(y[0], y[1]));
      if (r < 15) { e = cmul(e, e1); y = cmul(y, e16); }
    }
    f32x2 st = c0;
#pragma unroll
    for (int T = 1; T < 8; ++T) {
      st = cmul(st, e256);
      const int cnt = T == 1 ? 9 : (T < 4 ? 5 : 3);
      const int base = T == 1 ? 16 : (T < 4 ? 25 + 5 * (T - 2) : 35 + 3 * (T - 4));
      const f32x2 step = T == 1 ? e2 : (T < 4 ? e4 : e8);
      f32x2 v = st;
#pragma unroll
      for (int q = 0; q < cnt; ++q) { PUTX(base + q, v); if (q + 1 < cnt) v = cmul(v, step); }
    }
  }
#undef PUTX
  __syncthreads();
  f16x8 yb[8];
#pragma unroll
  for (int ks = 0; ks < 8; ++ks) yb[ks] = lds_ld<f16x8>(img + (47 + l16) * XROW + 16 * g + 64 * ks);
  const float sc = (2.f * dt) * (s * (1.f / 268435456.f));
  const int rho = 15 - l16;
#pragma unroll
  for (int tt = 0; tt < 4; ++tt) {
    int xi;
    if (sw & 1) xi = 35 + 3 * tt + ((rho + 3 + ((rho >> 3) & 1)) >> 3);
    else if (tt == 0) xi = rho;
    else if (tt == 1) xi = 16 + ((rho + ((rho >> 1) & 1)) >> 1);
    else xi = 25 + 5 * (tt - 2) + ((rho + 1 + ((rho >> 2) & 1)) >> 2);
    lptr ap = img + xi * XROW + 16 * g;
    f32x4 acc = {0.f, 0.f, 0.f, 0.f};
#pragma unroll
    for (int ks = 0; ks < 8; ++ks) acc = __builtin_amdgcn_mfma_f32_16x16x32_f16(lds_ld<f16x8>(ap + 64 * ks), yb[ks], acc, 0, 0, 0);
    kout[tt][0] = pkbf(acc[0] * sc, acc[1] * sc);
    kout[tt][1] = pkbf(acc[2] * sc, acc[3] * sc);
  }
}

__global__ __launch_bounds__(768) void k_prep(Params p) {
  __shared__ __attribute__((aligned(16))) char smem[LA_BYTES];
  lptr lds = (lptr)smem;
  const int b = blockIdx.x, m = 32 * (b & 7) + (b >> 3);
  const int w = __builtin_amdgcn_readfirstlane(threadIdx.x >> 6), lane = threadIdx.x & 63;
  if (w < 8) {
    phaseA_proj(p, lds, m, w, lane);
  } else {
    u32x2 kout[4];
    phaseA_svc(p, lds, m, w - 8, lane, kout);
    char* dst = p.ws + WS_KAP + ((size_t)(m * 4 + (w - 8)) * 64 + lane) * 32;
    u32x4 o0 = {kout[0][0], kout[0][1], kout[1][0], kout[1][1]}, o1 = {kout[2][0], kout[2][1], kout[3][0], kout[3][1]};
    gst<u32x4>(dst, o0);
    gst<u32x4>(dst + 16, o1);
    if (w == 8 && lane == 0) gst<u32>(p.ws + WS_FLAG + m * 4, 0u);
  }
}

constexpr int PW = 2560;
constexpr int LB_PART = 0, PBUF = 8 * PW;
constexpr int LB_L = LB_PART + 2 * PBUF, LBUF = 1024;
constexpr int LB_O = LB_L + 2 * LBUF, OROW = 144;
constexpr int LB_R = LB_O + 8 * OROW, RCOPY = 4160;
constexpr int LB_U = LB_R + 4 * RCOPY, UCH = 4608;
constexpr int LB_MX = LB_U + 2 * UCH;
constexpr int LB_G = LB_MX + 1024;
constexpr int LB_CTL = LB_G + 128;
constexpr int LB_DUMMY = LB_CTL + 16;
constexpr int LB_BYTES = LB_DUMMY + 1024 + 128;

DI void blk_barrier() { asm volatile("s_waitcnt lgkmcnt(0)\n\ts_barrier" ::: "memory"); }

DI f32x16 pv_tile(f32x16 o, bf16x8 v0, bf16x8 v1, const f32x16& pr) {
  f32x8 lo = __builtin_shufflevector(pr, pr, 0, 1, 2, 3, 4, 5, 6, 7);
  f32x8 hi = __builtin_shufflevector(pr, pr, 8, 9, 10, 11, 12, 13, 14, 15);
  o = __builtin_amdgcn_smfmac_f32_32x32x32_bf16(v0, __builtin_bit_cast(bf16x16, lo), o, 0xDDDD, 0, 0);
  o = __builtin_amdgcn_smfmac_f32_32x32x32_bf16(v1, __builtin_bit_cast(bf16x16, hi), o, 0xDDDD, 0, 0);
  return o;
}
DI void put_partial(lptr lds, int buf, int w, int lane, const f32x16& o) {
  lptr pp = lds + LB_PART + buf * PBUF + w * PW + (lane >> 5) * 640 + (lane & 31) * 16;
  f32x4 a = {o[0], o[1], o[2], o[3]}, b = {o[4], o[5], o[6], o[7]};
  lds_st<f32x4>(pp, a);
  lds_st<f32x4>(pp + 1280, b);
  if (lane < 32) lds_st<float>(lds + LB_L + buf * LBUF + lane * 32 + w * 4, o[8]);
}

#define ST_(x) #x
#define ST(x) ST_(x)
#define EX1(b, i) "v_exp_f32 v[" ST(b) "+" ST(i) "], v[" ST(b) "+" ST(i) "]\n"
#define EX_0_2(b) EX1(b, 0) EX1(b, 1)
#define EX_2_5(b) EX1(b, 2) EX1(b, 3) EX1(b, 4)
#define EX_5_10(b) EX1(b, 5) EX1(b, 6) EX1(b, 7) EX1(b, 8) EX1(b, 9)
#define EX_10_13(b) EX1(b, 10) EX1(b, 11) EX1(b, 12)
#define EX_13_16(b) EX1(b, 13) EX1(b, 14) EX1(b, 15)
#define PV(vi, pb) "v_smfmac_f32_32x32x32_bf16 v[0:15], v[80+4*" ST(vi) ":83+4*" ST(vi) "], v[" ST(pb) ":" ST(pb) "+7], v156\n"
#define QK(d, kt, q) "v_mfma_f32_32x32x16_bf16 v[" ST(d) ":" ST(d) "+15], v[48+4*" ST(kt) ":51+4*" ST(kt) "], v[" ST(q) ":" ST(q) "+3], 0\n"
#define TILE(W, C, N, vprev, vcur, kn, q) W EX_0_2(C) PV(vprev, N+8) EX_2_5(C) QK(N, kn, q) EX_5_10(C) PV(vcur, C) EX_10_13(C) EX_13_16(C)
#define PUT(po, lo) "ds_write_b128 %[pa], v[0:3] offset:" ST(po) "\n ds_write_b128 %[pa], v[4:7] offset:" ST(po) "+1280\n ds_write_b32 %[la], v8 offset:" ST(lo) "\n"
#define ROUND(q, qn, po, lo, W0, W1, W2, W3, W4, W5, W6) \
  "buffer_load_dwordx4 v[" ST(qn) ":" ST(qn) "+3], %[lo16], %[rs], %[qs] offen\n s_add_u32 %[qs], %[qs], 1024\n" \
  W0 EX_0_2(16) PV(15, 40) EX_2_5(16) QK(32, 1, q) EX_5_10(16) EX_10_13(16) PUT(po, lo) \
  "v_mfma_f32_32x32x16_bf16 v[0:15], v[152:155], v[" ST(q) ":" ST(q) "+3], 0\n" EX_13_16(16) PV(0, 16) \
  TILE(W1, 32, 16, 1, 2, 2, q) TILE(W2, 16, 32, 3, 4, 3, q) "s_waitcnt lgkmcnt(0)\n s_barrier\n" \
  TILE(W3, 32, 16, 5, 6, 4, q) TILE(W4, 16, 32, 7, 8, 5, q) TILE(W5, 32, 16, 9, 10, 6, q) TILE(W6, 16, 32, 11, 12, 7, q) \
  TILE("s_waitcnt vmcnt(0)\n", 32, 16, 13, 14, 0, qn)
#define VM(n) "s_waitcnt vmcnt(" ST(n) ")\n"
#define LDK(t, so, off) "buffer_load_dwordx4 v[48+4*" ST(t) ":51+4*" ST(t) "], %[lo16], %[rs], " so " offen offset:" ST(off) "\n"
#define LDV(i, so, off) "buffer_load_dwordx4 v[80+4*" ST(i) ":83+4*" ST(i) "], %[vo16], %[rs], " so " offen offset:" ST(off) "\n"
#define XV "s_mov_b64 exec, %[vm]\n"
#define XA "s_mov_b64 exec, -1\n"
#define FILL4(i) "v_mov_b32 v[80+" ST(i) "], %[fill]\n v_mov_b32 v[81+" ST(i) "], %[fill]\n v_mov_b32 v[82+" ST(i) "], %[fill]\n v_mov_b32 v[83+" ST(i) "], %[fill]\n"
#define FILL16(i) FILL4(i) FILL4(i+4) FILL4(i+8) FILL4(i+12)
#define CL10(a) "v" ST(a) "0", "v" ST(a) "1", "v" ST(a) "2", "v" ST(a) "3", "v" ST(a) "4", "v" ST(a) "5", "v" ST(a) "6", "v" ST(a) "7", "v" ST(a) "8", "v" ST(a) "9"
static_assert(PBUF == 20480 && LBUF == 1024, "offsets are spelled out in the asm");

DI void attn_role(const Params& p, lptr lds, int m, int w, int lane) {
  const int r32 = lane & 31, h = lane >> 5;
  const rsrc_t rs = ws_rsrc(p.ws);
  const u32 kp = (u32)WS_KF + w * 8192, vp = (u32)WS_VF + w * 8192, qp = (u32)WS_QF + m * 8192;
  const u32 lo16 = lane * 16, vo16 = (h * 16 + (r32 & 15)) * 16;
  const u32 fill = (r32 == 16) ? 0x3f803f80u : 0u;
  {
    const u32 pa = (u32)(uintptr_t)(lds + LB_PART + w * PW + h * 640 + r32 * 16);
    const u32 la = (u32)(uintptr_t)(lds + (h ? LB_DUMMY + r32 * 4 : LB_L + r32 * 32 + w * 4));
    u32 qs = qp, cnt = 4;
    const u64 vm = 0x0000ffff0000ffffull;
    asm volatile(
      "s_nop 4\n"
      "buffer_load_dwordx4 v[144:147], %[lo16], %[rs], %[qs] offen\n s_add_u32 %[qs], %[qs], 1024\n"
      LDK(0, "%[k0]", 0) LDK(1, "%[k0]", 1024) XV LDV(0, "%[v0]", 0) LDV(1, "%[v0]", 512) XA
      LDK(2, "%[k0]", 2048) XV LDV(2, "%[v0]", 1024) LDV(3, "%[v0]", 1536) XA
      LDK(3, "%[k0]", 3072) XV LDV(4, "%[v0]", 2048) LDV(5, "%[v0]", 2560) XA
      LDK(4, "%[k1]", 0) XV LDV(6, "%[v0]", 3072) LDV(7, "%[v0]", 3584) XA
      LDK(5, "%[k1]", 1024) XV LDV(8, "%[v1]", 0) LDV(9, "%[v1]", 512) XA
      LDK(6, "%[k1]", 2048) XV LDV(10, "%[v1]", 1024) LDV(11, "%[v1]", 1536) XA
      LDK(7, "%[k1]", 3072) XV LDV(12, "%[v1]", 2048) LDV(13, "%[v1]", 2560) LDV(14, "%[v1]", 3072) LDV(15, "%[v1]", 3584)
      "s_not_b64 exec, exec\n"
      FILL16(0) FILL16(16) FILL16(32) FILL16(48)
      XA
      "v_mov_b32 v152, 0\n v_mov_b32 v153, 0\n v_mov_b32 v154, 0\n v_mov_b32 v155, 0\n v_mov_b32 v156, 0xdddd\n"
      "s_waitcnt vmcnt(23)\n"
      QK(16, 0, 144)
      "s_nop 9\n"
      "1:\n"
      ROUND(144, 148, 20480, 1024, VM(22), VM(19), VM(16), VM(13), VM(10), VM(7), VM(4))
      ROUND(148, 144, 0, 0, "", "", "", "", "", "", "")
      "s_sub_u32 %[cnt], %[cnt], 1\n s_cmp_lg_u32 %[cnt], 0\n s_cbranch_scc1 1b\n"
      PV(15, 40)
      "s_nop 11\n"
      PUT(20480, 1024)
      "s_waitcnt lgkmcnt(0)\n s_barrier\n"
      : [qs] "+s"(qs), [cnt] "+s"(cnt)
      : [lo16] "v"(lo16), [vo16] "v"(vo16), [pa] "v"(pa), [la] "v"(la), [fill] "v"(fill), [rs] "s"(rs),
        [k0] "s"(kp), [k1] "s"(kp + 4096), [v0] "s"(vp), [v1] "s"(vp + 4096), [vm] "s"(vm)
      : "memory", "scc", CL10(), CL10(1), CL10(2), CL10(3), CL10(4), CL10(5), CL10(6), CL10(7), CL10(8), CL10(9),
        CL10(10), CL10(11), CL10(12), CL10(13), CL10(14), "v150", "v151", "v152", "v153", "v154", "v155", "v156");
  }
  blk_barrier();
  if (__builtin_amdgcn_readfirstlane(lds_ld<u32>(lds + LB_CTL + 4)) != 0u) {
    const f32x16 zero16 = {0.f, 0.f, 0.f, 0.f, 0.f, 0.f, 0.f, 0.f, 0.f, 0.f, 0.f, 0.f, 0.f, 0.f, 0.f, 0.f};
    const u32x4 fill4 = {fill, fill, fill, fill};
#pragma unroll 1
    for (int r = 0; r < 8; ++r) {
      const bf16x8 qf = bld8(rs, lo16, qp + r * 1024);
      float mx = -INFINITY;
#pragma unroll 1
      for (int t = 0; t < 8; ++t) {
        f32x16 s = mfma32(bld8(rs, lo16, kp + t * 1024), qf, zero16);
#pragma unroll
        for (int e = 0; e < 16; ++e) mx = fmaxf(mx, s[e]);
      }
      mx = fmaxf(mx, __shfl_xor(mx, 32));
      if (lane < 32) lds_st<float>(lds + LB_MX + lane * 32 + w * 4, mx);
      blk_barrier();
      f32x4 m0 = lds_ld<f32x4>(lds + LB_MX + r32 * 32), m1 = lds_ld<f32x4>(lds + LB_MX + r32 * 32 + 16);
      mx = fmaxf(fmaxf(fmaxf(m0[0], m0[1]), fmaxf(m0[2], m0[3])), fmaxf(fmaxf(m1[0], m1[1]), fmaxf(m1[2], m1[3])));
      f32x16 o = zero16;
#pragma unroll 1
      for (int t = 0; t < 8; ++t) {
        f32x16 s = mfma32(bld8(rs, lo16, kp + t * 1024), qf, zero16);
        u32x4 v0 = bld4(rs, vo16, vp + t * 1024), v1 = bld4(rs, vo16, vp + t * 1024 + 512);
        if (r32 >= 16) { v0 = fill4; v1 = fill4; }
#pragma unroll
        for (int e = 0; e < 16; ++e) s[e] = __builtin_amdgcn_exp2f(s[e] - mx);
        o = pv_tile(o, __builtin_bit_cast(bf16x8, v0), __builtin_bit_cast(bf16x8, v1), s);
      }
      put_partial(lds, r & 1, w, lane, o);
      blk_barrier();
    }
  }
}

template <int SET> DI void conv_part(lptr fp, lptr gp, int r, f32x4 (&acc)[4]) {
  constexpr int NB[4] = {SET ? 1 : 0, SET ? 2 : 3, SET ? 5 : 4, SET ? 6 : 7};
  constexpr int MX = NB[3];
  bf16x8 gf[MX + 1];
  lptr g0 = gp - 64 * r, f0 = fp - 64 * r;
#pragma unroll
  for (int mm = 0; mm <= MX; ++mm) gf[mm] = lds_ld<bf16x8>(g0 + 512 * mm);
#pragma unroll
  for (int e = 0; e <= MX; ++e) {
    lptr fa = f0 + 512 * (7 - e);
    u32x4 fv = {lds_ld<u32>(fa), lds_ld<u32>(fa + 4), lds_ld<u32>(fa + 8), lds_ld<u32>(fa + 12)};
    const bf16x8 ff = __builtin_bit_cast(bf16x8, fv);
#pragma unroll
    for (int a = 0; a < 4; ++a)
      if (NB[a] >= e) acc[a] = mfma16(ff, gf[NB[a] - e], acc[a]);
  }
}

DI void svc_sync(lptr lds, u32 target) {
  asm volatile("s_waitcnt lgkmcnt(0)" ::: "memory");
  LAS u32* c = (LAS u32*)(lds + LB_CTL);
  if ((threadIdx.x & 63) == 0) __hip_atomic_fetch_add(c, 1u, __ATOMIC_RELAXED, __HIP_MEMORY_SCOPE_WORKGROUP);
  while (__builtin_amdgcn_readfirstlane(__hip_atomic_load(c, __ATOMIC_RELAXED, __HIP_MEMORY_SCOPE_WORKGROUP)) < target) __builtin_amdgcn_s_sleep(1);
  asm volatile("" ::: "memory");
}

DI void load_wo(rsrc_t rs, u32 lo16, u32 wp, int r, u32x8 (&wf)[8]) {
#pragma unroll
  for (int q = 0; q < 8; ++q) {
    u32x4 a = bld4(rs, lo16, wp + r * 65536 + q * 2048), b = bld4(rs, lo16, wp + r * 65536 + q * 2048 + 1024);
    wf[q] = __builtin_shufflevector(a, b, 0, 1, 2, 3, 4, 5, 6, 7);
  }
}

DI void svc_round(lptr lds, int buf, int sw, int lane, u32 target, const u32x8 (&wf)[8], f32x4 (&oacc)[4], bool& bad) {
  const int tt = lane & 7, dp = lane >> 3;
  lptr pp = lds + LB_PART + buf * PBUF + (dp >> 1) * 640 + (8 * sw + tt) * 16 + 8 * (dp & 1);
  f32x2 v[8];
#pragma unroll
  for (int w = 0; w < 8; ++w) v[w] = lds_ld<f32x2>(pp + w * PW);
  const f32x4 l0 = lds_ld<f32x4>(lds + LB_L + buf * LBUF + (8 * sw + tt) * 32), l1 = lds_ld<f32x4>(lds + LB_L + buf * LBUF + (8 * sw + tt) * 32 + 16);
  const f32x2 s = ((v[0] + v[1]) + (v[2] + v[3])) + ((v[4] + v[5]) + (v[6] + v[7]));
  const f32x4 l4 = l0 + l1;
  const float l = (l4[0] + l4[1]) + (l4[2] + l4[3]);
  const float rl = __builtin_amdgcn_rcpf(l);
  const float o0 = s[0] * rl, o1 = s[1] * rl;
  bad = bad || !(l > 8.7e-19f && l < 1.1e30f && fabsf(s[0]) < 3e38f && fabsf(s[1]) < 3e38f);
  lds_st<u32>(lds + LB_O + tt * OROW + 32 * sw + 4 * dp, pkbf(o0, o1));
  svc_sync(lds, target);
  const int l16 = lane & 15, g = lane >> 4;
  const int idx = (lane & 1) ? 0xDDDD : 0x8888;
  lptr ap = lds + LB_O + (l16 >> 1) * OROW + 16 * g;
  const bf16x8 a0 = lds_ld<bf16x8>(ap), a1 = lds_ld<bf16x8>(ap + 64);
#pragma unroll
  for (int nt = 0; nt < 4; ++nt) oacc[nt] = __builtin_amdgcn_smfmac_f32_16x16x64_bf16(a0, __builtin_bit_cast(bf16x16, wf[nt]), oacc[nt], idx, 0, 0);
#pragma unroll
  for (int nt = 0; nt < 4; ++nt) oacc[nt] = __builtin_amdgcn_smfmac_f32_16x16x64_bf16(a1, __builtin_bit_cast(bf16x16, wf[4 + nt]), oacc[nt], idx, 0, 0);
}

DI void store_out(const Params& p, int m, int sw, int lane, const f32x4 (&oacc)[4], const float (&pre)[16], float g0, float g1) {
  const int l16 = lane & 15, g = lane >> 4;
  u32x4 o0, o1;
#pragma unroll
  for (int nt = 0; nt < 4; ++nt) {
    o0[nt] = pkbf(__builtin_fmaf(g0, oacc[nt][0], pre[2 * nt]), __builtin_fmaf(g0, oacc[nt][1], pre[2 * nt + 1]));
    o1[nt] = pkbf(__builtin_fmaf(g1, oacc[nt][2], pre[8 + 2 * nt]), __builtin_fmaf(g1, oacc[nt][3], pre[8 + 2 * nt + 1]));
  }
  u16* dst = p.out + (size_t)(8 * m + 2 * g) * DM + 128 * sw + 8 * l16;
  gst<u32x4>(dst, o0);
  gst<u32x4>(dst + DM, o1);
}

DI void svc_role(const Params& p, lptr lds, int m, int sw, int lane) {
  __builtin_amdgcn_s_setprio(2);
  const int l16 = lane & 15, g = lane >> 4;
  const int cl = sw >> 1, ch = 2 * m + cl;
  const char* ksrc = p.ws + WS_KAP + ((size_t)(m * 4 + sw) * 64 + lane) * 32;
  const u32x4 k0 = gld<u32x4>(ksrc), k1 = gld<u32x4>(ksrc + 16);
  const char* usrc = p.ws + WS_XT + (size_t)ch * (SEQ * 2) + (sw & 1) * 2048 + lane * 16;
  const u32x4 u0 = gld<u32x4>(usrc), u1 = gld<u32x4>(usrc + 1024);
  const u16* xsrc = p.x + (size_t)(8 * m + 2 * g) * DM + 128 * sw + 8 * l16;
  {
    const u32x4 x0 = gld<u32x4>(xsrc), x1 = gld<u32x4>(xsrc + DM), gw = gld<u32x4>(p.gatew + 128 * sw + 8 * l16);
    float s0 = 0.f, s1 = 0.f;
#pragma unroll
    for (int e = 0; e < 4; ++e) {
      const u32 p0 = pkbf(bflo(x0[e]) * bflo(gw[e]), bfhi(x0[e]) * bfhi(gw[e]));
      const u32 p1 = pkbf(bflo(x1[e]) * bflo(gw[e]), bfhi(x1[e]) * bfhi(gw[e]));
      s0 += bflo(p0) + bfhi(p0);
      s1 += bflo(p1) + bfhi(p1);
    }
#pragma unroll
    for (int o = 8; o >= 1; o >>= 1) { s0 += __shfl_xor(s0, o); s1 += __shfl_xor(s1, o); }
    if (l16 == 0) { lds_st<float>(lds + LB_G + (2 * g) * 16 + sw * 4, s0); lds_st<float>(lds + LB_G + (2 * g + 1) * 16 + sw * 4, s1); }
  }
  const rsrc_t rs = ws_rsrc(p.ws);
  const u32 wp = (u32)WS_WOF + sw * 16384, lo16 = lane * 16;
  u32x8 wf[8];
  {
    lptr r0 = lds + LB_R + cl * (2 * RCOPY), r1 = r0 + RCOPY;
    const u32 kk[8] = {k0[0], k0[1], k0[2], k0[3], k1[0], k1[1], k1[2], k1[3]};
#pragma unroll
    for (int tt = 0; tt < 4; ++tt) {
      const int i0 = 2032 - 256 * (4 * (sw & 1) + tt) - 16 * l16 + 4 * g;
      const u32 a = kk[2 * tt], b = kk[2 * tt + 1];
      u32x2 ab = {a, b};
      lds_st<u32x2>(r0 + i0 * 2, ab);
      if (i0 > 0) lds_st<u16>(r1 + i0 * 2 - 2, (u16)a);
      lds_st<u32>(r1 + i0 * 2, (a >> 16) | (b << 16));
      lds_st<u16>(r1 + i0 * 2 + 4, (u16)(b >> 16));
    }
    lptr ub = lds + LB_U + cl * UCH;
    lds_st<u32x4>(ub + 512 + (sw & 1) * 2048 + lane * 16, u0);
    lds_st<u32x4>(ub + 512 + (sw & 1) * 2048 + 1024 + lane * 16, u1);
    if ((sw & 1) == 0) {
      u32x2 z = {0u, 0u};
      lds_st<u32x2>(ub + lane * 8, z);
      if (lane < 16) { lds_st<u32>(r0 + 4096 + lane * 4, 0u); lds_st<u32>(r1 + 4096 + lane * 4, 0u); }
      if (lane == 0) lds_st<u16>(r1 + 4094, (u16)0);
    }
    if (sw == 0 && lane < 2) lds_st<u32>(lds + LB_CTL + lane * 4, 0u);
  }
  lptr fp = lds + LB_R + cl * (2 * RCOPY) + ((l16 & 1) ? 0 : RCOPY) + (1015 - 16 * 56 - (l16 >> 1) + 4 * g) * 4;
  lptr gp = lds + LB_U + cl * UCH + 512 + 32 * l16 - 32 + 16 * g;
  f32x4 oacc[4], cacc[4];
#pragma unroll
  for (int a = 0; a < 4; ++a) { oacc[a] = f32x4{0.f, 0.f, 0.f, 0.f}; cacc[a] = oacc[a]; }
  bool bad = false;
#pragma unroll 1
  for (int s = 0; s <= 3; ++s) {
    blk_barrier();
    if (s > 0) svc_round(lds, (s - 1) & 1, sw, lane, 4u * s, wf, oacc, bad);
    if (sw & 1) { conv_part<1>(fp, gp, 2 * s, cacc); conv_part<1>(fp, gp, 2 * s + 1, cacc); }
    else { conv_part<0>(fp, gp, 2 * s, cacc); conv_part<0>(fp, gp, 2 * s + 1, cacc); }
    load_wo(rs, lo16, wp, s, wf);
  }
  {
    char* yd = p.ws + WS_YT + (size_t)ch * (SEQ * 2) + (16 * l16 + 4 * g) * 2;
#pragma unroll
    for (int a = 0; a < 4; ++a) {
      const int nb = (sw & 1) ? (a == 0 ? 1 : a == 1 ? 2 : a == 2 ? 5 : 6) : (a == 0 ? 0 : a == 1 ? 3 : a == 2 ? 4 : 7);
      const u64 v = (u64)pkbf(cacc[a][0], cacc[a][1]) | ((u64)pkbf(cacc[a][2], cacc[a][3]) << 32);
      __hip_atomic_store((GAS u64*)(yd + nb * 512), v, RLX_AGENT);
    }
  }
#pragma unroll 1
  for (int s = 4; s <= 7; ++s) {
    blk_barrier();
    if (s == 5 && sw == 0 && lane == 0) __hip_atomic_store((GAS u32*)(p.ws + WS_FLAG + m * 4), 1u, RLX_AGENT);
    svc_round(lds, (s - 1) & 1, sw, lane, 4u * s, wf, oacc, bad);
    asm volatile("s_waitcnt vmcnt(0)" ::: "memory");
    load_wo(rs, lo16, wp, s, wf);
  }
  float pre[16], g0, g1;
  {
    GAS u32* fl = (GAS u32*)(p.ws + WS_FLAG) + 64 * sw + lane;
#pragma unroll 1
    for (u32 spins = 0; spins < (1u << 22); ++spins) {
      if (__all(__hip_atomic_load(fl, RLX_AGENT) == 1u)) break;
      __builtin_amdgcn_s_sleep(2);
    }
    __builtin_amdgcn_fence(__ATOMIC_ACQUIRE, "wavefront");
    const char* ys = p.ws + WS_YT + (size_t)(128 * sw + 8 * l16) * (SEQ * 2) + (8 * m + 2 * g) * 2;
    u32 yv[8];
#pragma unroll
    for (int e = 0; e < 8; ++e) yv[e] = __hip_atomic_load((GAS u32*)(ys + (size_t)e * (SEQ * 2)), RLX_AGENT);
    const u32x4 x0 = gld<u32x4>(xsrc), x1 = gld<u32x4>(xsrc + DM), dk = gld<u32x4>(p.Dskip + 128 * sw + 8 * l16);
    const f32x4 ga = lds_ld<f32x4>(lds + LB_G + (2 * g) * 16), gb = lds_ld<f32x4>(lds + LB_G + (2 * g + 1) * 16);
    const float z0 = bfr((ga[0] + ga[1]) + (ga[2] + ga[3])), z1 = bfr((gb[0] + gb[1]) + (gb[2] + gb[3]));
    g0 = bfr(1.f / (1.f + __expf(-z0)));
    g1 = bfr(1.f / (1.f + __expf(-z1)));
    const float h0 = bfr(1.f - g0), h1 = bfr(1.f - g1);
#pragma unroll
    for (int e = 0; e < 4; ++e) {
      pre[2 * e] = h0 * __builtin_fmaf(bflo(x0[e]), bflo(dk[e]), bflo(yv[2 * e]));
      pre[2 * e + 1] = h0 * __builtin_fmaf(bfhi(x0[e]), bfhi(dk[e]), bflo(yv[2 * e + 1]));
      pre[8 + 2 * e] = h1 * __builtin_fmaf(bflo(x1[e]), bflo(dk[e]), bfhi(yv[2 * e]));
      pre[8 + 2 * e + 1] = h1 * __builtin_fmaf(bfhi(x1[e]), bfhi(dk[e]), bfhi(yv[2 * e + 1]));
    }
  }
  blk_barrier();
  svc_round(lds, 1, sw, lane, 32u, wf, oacc, bad);
  store_out(p, m, sw, lane, oacc, pre, g0, g1);
  if (__any(bad) && lane == 0) lds_st<u32>(lds + LB_CTL + 4, 1u);
  blk_barrier();
  if (__builtin_amdgcn_readfirstlane(lds_ld<u32>(lds + LB_CTL + 4)) != 0u) {
#pragma unroll
    for (int a = 0; a < 4; ++a) oacc[a] = f32x4{0.f, 0.f, 0.f, 0.f};
#pragma unroll 1
    for (int r = 0; r < 8; ++r) {
      load_wo(rs, lo16, wp, r, wf);
      blk_barrier();
      blk_barrier();
      svc_round(lds, r & 1, sw, lane, 36u + 4u * r, wf, oacc, bad);
    }
    store_out(p, m, sw, lane, oacc, pre, g0, g1);
  }
}

__global__ __launch_bounds__(768) void k_main(Params p) {
  __shared__ __attribute__((aligned(16))) char smem[LB_BYTES];
  lptr lds = (lptr)smem;
  const int b = blockIdx.x, m = 32 * (b & 7) + (b >> 3);
  const int w = __builtin_amdgcn_readfirstlane(threadIdx.x >> 6), lane = threadIdx.x & 63;
  if (w < 8) attn_role(p, lds, m, w, lane);
  else svc_role(p, lds, m, w - 8, lane);
}

extern "C" void kernel_launch(void* const* d_in, const int* in_sizes, int n_in, void* d_out, int out_size,
                              void* d_ws, size_t ws_size, hipStream_t stream) {
  Params p;
  p.x = (const u16*)d_in[0]; p.wq = (const u16*)d_in[1]; p.wk = (const u16*)d_in[2]; p.wv = (const u16*)d_in[3];
  p.wo = (const u16*)d_in[4]; p.logA = (const u16*)d_in[6]; p.Aim = (const u16*)d_in[7]; p.Cre = (const u16*)d_in[8];
  p.Cim = (const u16*)d_in[9]; p.logdt = (const u16*)d_in[10]; p.Dskip = (const u16*)d_in[11]; p.gatew = (const u16*)d_in[12];
  p.out = (u16*)d_out; p.ws = (char*)d_ws;
  k_prep<<<256, 768, 0, stream>>>(p);
  k_main<<<256, 768, 0, stream>>>(p);
}
```

```cpp
#include <hip/hip_runtime.h>
#include <stdint.h>

typedef unsigned short u16;
typedef unsigned int u32;
typedef unsigned long long u64;
typedef __bf16 bf16x2 __attribute__((ext_vector_type(2)));
typedef __bf16 bf16x8 __attribute__((ext_vector_type(8)));
typedef __bf16 bf16x16 __attribute__((ext_vector_type(16)));
typedef _Float16 f16x2 __attribute__((ext_vector_type(2)));
typedef _Float16 f16x8 __attribute__((ext_vector_type(8)));
typedef float f32x2 __attribute__((ext_vector_type(2)));
typedef float f32x4 __attribute__((ext_vector_type(4)));
typedef float f32x8 __attribute__((ext_vector_type(8)));
typedef float f32x16 __attribute__((ext_vector_type(16)));
typedef u32 u32x2 __attribute__((ext_vector_type(2)));
typedef u32 u32x4 __attribute__((ext_vector_type(4)));
typedef u32 u32x8 __attribute__((ext_vector_type(8)));
typedef short s16x4 __attribute__((ext_vector_type(4)));
#define LAS __attribute__((address_space(3)))
#define GAS __attribute__((address_space(1)))
#define DI __device__ __forceinline__
#define RLX_AGENT __ATOMIC_RELAXED, __HIP_MEMORY_SCOPE_AGENT
#define RLX_WG __ATOMIC_RELAXED, __HIP_MEMORY_SCOPE_WORKGROUP
typedef LAS char* lptr;

constexpr int SEQ = 2048, DM = 512, NMODE = 128;
constexpr u32 WS_EP = 0;
constexpr u32 FS = 128;
constexpr u32 WS_F1 = 4096;
constexpr u32 WS_F2 = WS_F1 + 256 * FS;
constexpr u32 WS_F3 = WS_F2 + 256 * FS;
constexpr u32 WS_KF = 131072;
constexpr u32 WS_VF = WS_KF + 65536;
constexpr u32 WS_WOF = WS_VF + 65536;
constexpr u32 WS_QF = WS_WOF + 524288;
constexpr u32 WS_XT = WS_QF + 2097152;
constexpr u32 WS_YT = WS_XT + 2097152;
constexpr u32 WS_BYTES = WS_YT + 2097152;

struct Params {
  const u16 *x, *wq, *wk, *wv, *wo, *logA, *Aim, *Cre, *Cim, *logdt, *Dskip, *gatew;
  u16* out;
  char* ws;
};

#define CSTAMP(id) do {} while (0)
#define STAMP(id) do {} while (0)
#define STAMP_INIT(w_) do {} while (0)
#define STAMP_DUMP() do {} while (0)
constexpr int STAMP_BYTES = 0;
DI float bf2f(u16 v) { return __uint_as_float((u32)v << 16); }
DI float bflo(u32 v) { return __uint_as_float(v << 16); }
DI float bfhi(u32 v) { return __uint_as_float(v & 0xffff0000u); }
DI u32 pkbf(float a, float b) { f32x2 v = {a, b}; return __builtin_bit_cast(u32, __builtin_convertvector(v, bf16x2)); }
DI float bfr(float a) { return bflo(pkbf(a, a)); }
DI u32 pkh(f32x2 v) { return __builtin_bit_cast(u32, __builtin_convertvector(v, f16x2)); }
DI f32x4 mfma16(bf16x8 a, bf16x8 b, f32x4 c) { return __builtin_amdgcn_mfma_f32_16x16x32_bf16(a, b, c, 0, 0, 0); }
DI f32x16 mfma32(bf16x8 a, bf16x8 b, f32x16 c) { return __builtin_amdgcn_mfma_f32_32x32x16_bf16(a, b, c, 0, 0, 0); }
template <class T> DI T lds_ld(lptr p) { return *(LAS T*)p; }
template <class T> DI void lds_st(lptr p, T v) { *(LAS T*)p = v; }
template <class T> DI T gld(const void* p) { return *(const GAS T*)p; }
template <class T> DI void gst(void* p, T v) { *(GAS T*)p = v; }
typedef __amdgpu_buffer_rsrc_t rsrc_t;
DI rsrc_t ws_rsrc(char* ws) { return __builtin_amdgcn_make_buffer_rsrc(ws, 0, WS_BYTES, 0x00020000); }
DI u32x4 bld4(rsrc_t r, u32 voff, u32 soff) { return __builtin_amdgcn_raw_buffer_load_b128(r, voff, soff, 16); }
DI u32x2 bld2(rsrc_t r, u32 voff, u32 soff) { return __builtin_amdgcn_raw_buffer_load_b64(r, voff, soff, 16); }
DI u32 bld1(rsrc_t r, u32 voff, u32 soff) { return __builtin_amdgcn_raw_buffer_load_b32(r, voff, soff, 16); }
DI bf16x8 bld8(rsrc_t r, u32 voff, u32 soff) { return __builtin_bit_cast(bf16x8, bld4(r, voff, soff)); }
DI void bst4(rsrc_t r, u32 voff, u32 soff, u32x4 v) { __builtin_amdgcn_raw_buffer_store_b128(v, r, voff, soff, 16); }
DI void bst2(rsrc_t r, u32 voff, u32 soff, u32x2 v) { __builtin_amdgcn_raw_buffer_store_b64(v, r, voff, soff, 16); }
DI bf16x8 trfrag(lptr lo_p, lptr hi_p) {
  s16x4 lo = __builtin_amdgcn_ds_read_tr16_b64_v4i16((LAS s16x4*)lo_p);
  s16x4 hi = __builtin_amdgcn_ds_read_tr16_b64_v4i16((LAS s16x4*)hi_p);
  return __builtin_bit_cast(bf16x8, __builtin_shufflevector(lo, hi, 0, 1, 2, 3, 4, 5, 6, 7));
}
DI void drain_vm() { asm volatile("s_waitcnt vmcnt(0)" ::: "memory"); }
DI void blk_barrier() { asm volatile("s_waitcnt lgkmcnt(0)\n\ts_barrier" ::: "memory"); }

constexpr int L_WQ = 0, WSL = 16416;
constexpr int XROW = 528, XYCH = 63 * XROW;
constexpr int L_XY = 4 * WSL;
constexpr int L_KVP = L_XY + 2 * XYCH;
constexpr int L_EX = L_KVP + 8192;
constexpr int L_G = L_EX + 16384;
constexpr int L_CTL = L_G + 128;
constexpr int L_BYTES = L_CTL + 128;
constexpr int PW = 2560;
constexpr int L_PART = 0, PBUF = 8 * PW;
constexpr int L_L = L_PART + 2 * PBUF, LBUF = 1024;
constexpr int L_DUMMY = L_L + 2 * LBUF;
constexpr int L_O = L_DUMMY + 2 * LBUF, OROW = 144;
constexpr int L_R = L_O + 8 * OROW, RCOPY = 4160;
static_assert(L_R + 4 * RCOPY <= L_XY, "phase B images must fit in the wq image");
constexpr int L_U = L_XY, UCH = 4608;
constexpr int L_QR = L_U + 2 * UCH;
constexpr int L_MX = L_QR + 9216;
constexpr int L_YS = L_MX + 1024, YSW = 2304;
constexpr int C_SVC = 0, C_BAD = 4, C_KV = 8, C_WQ = 16  , C_XY = 24  , C_Q = 32  , C_EX = 64  ;

DI void cnt_add(lptr lds, int off) {
  asm volatile("s_waitcnt lgkmcnt(0)" ::: "memory");
  if ((threadIdx.x & 63) == 0) __hip_atomic_fetch_add((LAS u32*)(lds + L_CTL + off), 1u, RLX_WG);
}
DI void cnt_wait(lptr lds, int off, u32 target) {
#pragma unroll 1
  for (u32 spins = 0; spins < (1u << 22); ++spins) {
    if ((u32)__builtin_amdgcn_readfirstlane(__hip_atomic_load((LAS u32*)(lds + L_CTL + off), RLX_WG)) >= target) break;
    __builtin_amdgcn_s_sleep(1);
  }
  asm volatile("" ::: "memory");
}

DI f32x16 pv_tile(f32x16 o, bf16x8 v0, bf16x8 v1, const f32x16& pr) {
  f32x8 lo = __builtin_shufflevector(pr, pr, 0, 1, 2, 3, 4, 5, 6, 7);
  f32x8 hi = __builtin_shufflevector(pr, pr, 8, 9, 10, 11, 12, 13, 14, 15);
  o = __builtin_amdgcn_smfmac_f32_32x32x32_bf16(v0, __builtin_bit_cast(bf16x16, lo), o, 0xDDDD, 0, 0);
  o = __builtin_amdgcn_smfmac_f32_32x32x32_bf16(v1, __builtin_bit_cast(bf16x16, hi), o, 0xDDDD, 0, 0);
  return o;
}

#define ST_(x) #x
#define ST(x) ST_(x)
#define EX1(b, i) "v_exp_f32 v[" ST(b) "+" ST(i) "], v[" ST(b) "+" ST(i) "]\n"
#define EX_0_2(b) EX1(b, 0) EX1(b, 1)
#define EX_2_5(b) EX1(b, 2) EX1(b, 3) EX1(b, 4)
#define EX_5_10(b) EX1(b, 5) EX1(b, 6) EX1(b, 7) EX1(b, 8) EX1(b, 9)
#define EX_10_13(b) EX1(b, 10) EX1(b, 11) EX1(b, 12)
#define EX_13_16(b) EX1(b, 13) EX1(b, 14) EX1(b, 15)
#define PV(vi, pb) "v_smfmac_f32_32x32x32_bf16 v[0:15], v[80+4*" ST(vi) ":83+4*" ST(vi) "], v[" ST(pb) ":" ST(pb) "+7], v156\n"
#define QK(d, kt, q) "v_mfma_f32_32x32x16_bf16 v[" ST(d) ":" ST(d) "+15], v[48+4*" ST(kt) ":51+4*" ST(kt) "], v[" ST(q) ":" ST(q) "+3], 0\n"
#define ZO(q) "v_mfma_f32_32x32x16_bf16 v[0:15], v[152:155], v[" ST(q) ":" ST(q) "+3], 0\n"
#define TILE(W, C, N, vprev, vcur, kn, q) W EX_0_2(C) PV(vprev, N+8) EX_2_5(C) QK(N, kn, q) EX_5_10(C) PV(vcur, C) EX_10_13(C) EX_13_16(C)
#define PUT(po, lo) "ds_write_b128 %[pa], v[0:3] offset:" ST(po) "\n ds_write_b128 %[pa], v[4:7] offset:" ST(po) "+1280\n ds_write_b32 %[la], v8 offset:" ST(lo) "\n"
#define BAR "s_waitcnt lgkmcnt(0)\n s_barrier\n"
#define LG0 "s_waitcnt lgkmcnt(0)\n"
#define RDQ(qn, off) "ds_read_b128 v[" ST(qn) ":" ST(qn) "+3], v157 offset:" ST(off) "\n"
#define ROUND(q, qn, po, lo, qoff) \
  EX_0_2(16) PV(15, 40) EX_2_5(16) QK(32, 1, q) EX_5_10(16) EX_10_13(16) PUT(po, lo) ZO(q) EX_13_16(16) PV(0, 16) \
  TILE("", 32, 16, 1, 2, 2, q) TILE("", 16, 32, 3, 4, 3, q) BAR RDQ(qn, qoff) \
  TILE("", 32, 16, 5, 6, 4, q) TILE("", 16, 32, 7, 8, 5, q) TILE("", 32, 16, 9, 10, 6, q) TILE("", 16, 32, 11, 12, 7, q) \
  TILE(LG0, 32, 16, 13, 14, 0, qn)
#define VM(n) "s_waitcnt vmcnt(" ST(n) ")\n"
#define ROUND0 \
  VM(22) EX_0_2(16) EX_2_5(16) QK(32, 1, 144) EX_5_10(16) VM(20) PV(0, 16) EX_10_13(16) EX_13_16(16) \
  TILE(VM(18), 32, 16, 1, 2, 2, 144) TILE(VM(15), 16, 32, 3, 4, 3, 144) TILE(VM(12), 32, 16, 5, 6, 4, 144) \
  TILE(VM(9), 16, 32, 7, 8, 5, 144) TILE(VM(6), 32, 16, 9, 10, 6, 144) BAR RDQ(148, 1024) \
  TILE(VM(3), 16, 32, 11, 12, 7, 144) TILE(VM(0) LG0, 32, 16, 13, 14, 0, 148)
#define LDK(t, so, off) "buffer_load_dwordx4 v[48+4*" ST(t) ":51+4*" ST(t) "], %[lo16], %[rs], " so " offen offset:" ST(off) " sc1\n"
#define LDV(i, so, off) "buffer_load_dwordx4 v[80+4*" ST(i) ":83+4*" ST(i) "], %[vo16], %[rs], " so " offen offset:" ST(off) " sc1\n"
#define XV "s_mov_b64 exec, %[vm]\n"
#define XA "s_mov_b64 exec, -1\n"
#define LOADKV \
  LDK(0, "%[k0]", 0) LDK(1, "%[k0]", 1024) LDK(2, "%[k0]", 2048) XV LDV(0, "%[v0]", 0) LDV(1, "%[v0]", 512) LDV(2, "%[v0]", 1024) XA \
  LDK(3, "%[k0]", 3072) XV LDV(3, "%[v0]", 1536) LDV(4, "%[v0]", 2048) XA \
  LDK(4, "%[k1]", 0) XV LDV(5, "%[v0]", 2560) LDV(6, "%[v0]", 3072) XA \
  LDK(5, "%[k1]", 1024) XV LDV(7, "%[v0]", 3584) LDV(8, "%[v1]", 0) XA \
  LDK(6, "%[k1]", 2048) XV LDV(9, "%[v1]", 512) LDV(10, "%[v1]", 1024) XA \
  LDK(7, "%[k1]", 3072) XV LDV(11, "%[v1]", 1536) LDV(12, "%[v1]", 2048) LDV(13, "%[v1]", 2560) LDV(14, "%[v1]", 3072) LDV(15, "%[v1]", 3584) XA
#define FILL4(i) "v_mov_b32 v[80+" ST(i) "], v147\n v_mov_b32 v[81+" ST(i) "], v147\n v_mov_b32 v[82+" ST(i) "], v147\n v_mov_b32 v[83+" ST(i) "], v147\n"
#define FILL16(i) FILL4(i) FILL4(i+4) FILL4(i+8) FILL4(i+12)
#define POLL "v_lshlrev_b32 v146, 3, %[lo16]\n v_and_b32 v146, 0xf80, v146\n buffer_load_dwordx2 v[144:145], v146, %[rs], %[f1] offen sc1\n"
#define SPIN(lbl, areg, off, tgt) \
  "s_mov_b32 %[t2], 0\n" lbl ":\n ds_read_b32 v158, " areg " offset:" ST(off) "\n s_add_u32 %[t2], %[t2], 0x400\n s_cselect_b32 %[t3], 63, 0\n" \
  "s_waitcnt lgkmcnt(0)\n v_readfirstlane_b32 %[t0], v158\n s_or_b32 %[t0], %[t0], %[t3]\n" \
  "s_cmp_lt_u32 %[t0], " ST(tgt) "\n s_cbranch_scc1 " lbl "b\n"
#define TRD(n, s) "ds_read_b64_tr_b16 v[144+4*" ST(n) ":145+4*" ST(n) "], %[trlo] offset:16416*" ST(n) "+1024*" ST(s) "\n" \
                  "ds_read_b64_tr_b16 v[146+4*" ST(n) ":147+4*" ST(n) "], %[trhi] offset:16416*" ST(n) "+1024*" ST(s) "\n"
#define GM(n, s, c) "v_mfma_f32_16x16x32_bf16 v[4*" ST(n) ":4*" ST(n) "+3], v[144+4*" ST(n) ":147+4*" ST(n) "], v[16+4*" ST(s) ":19+4*" ST(s) "], " c "\n"
#define GC(n) "v[4*" ST(n) ":4*" ST(n) "+3]"
#define LG6 "s_waitcnt lgkmcnt(6)\n"
#define GSTEP0 LG6 GM(0, 0, "0") TRD(0, 1) LG6 GM(1, 0, "0") TRD(1, 1) LG6 GM(2, 0, "0") TRD(2, 1) LG6 GM(3, 0, "0") TRD(3, 1)
#define GSTEP(s, sn) LG6 GM(0, s, GC(0)) TRD(0, sn) LG6 GM(1, s, GC(1)) TRD(1, sn) LG6 GM(2, s, GC(2)) TRD(2, sn) LG6 GM(3, s, GC(3)) TRD(3, sn)
#define GSTEP7 LG6 GM(2, 7, GC(2)) "s_waitcnt lgkmcnt(4)\n" GM(3, 7, GC(3)) "s_waitcnt lgkmcnt(2)\n" GM(0, 7, GC(0)) LG0 GM(1, 7, GC(1))
#define GSTEP6 LG6 GM(0, 6, GC(0)) "s_waitcnt lgkmcnt(4)\n" GM(1, 6, GC(1)) "s_waitcnt lgkmcnt(2)\n" GM(2, 6, GC(2)) TRD(2, 7) "s_waitcnt lgkmcnt(2)\n" GM(3, 6, GC(3)) TRD(3, 7) TRD(0, 7) TRD(1, 7)
#define AST(n)
#define ASTCL
#define ASTOUT(n) do {} while (0)
#define CL10(a) "v" ST(a) "0", "v" ST(a) "1", "v" ST(a) "2", "v" ST(a) "3", "v" ST(a) "4", "v" ST(a) "5", "v" ST(a) "6", "v" ST(a) "7", "v" ST(a) "8", "v" ST(a) "9"
static_assert(PBUF == 20480 && LBUF == 1024 && WSL == 16416, "offsets are spelled out in the asm");
typedef u32 u32x32 __attribute__((ext_vector_type(32)));

DI void attn_role(const Params& p, lptr lds, int m, int w, int lane, u64 tag) {
  const int i = m >> 3, j = m & 7, T0 = 64 * i;
  const int tq = w & 3, kh = w >> 2, l16 = lane & 15, g = lane >> 4;
  const int r32 = lane & 31, h = lane >> 5;
  const int q4 = l16 >> 2, pp = l16 & 3;
  STAMP_INIT(w);
  STAMP(0);
  CSTAMP(30);
  const u16* wkv = ((j & 1) ? p.wv : p.wk) + (size_t)w * 1024 + lane * 8;
  const u32x4 rkv0 = gld<u32x4>(wkv), rkv1 = gld<u32x4>(wkv + 512);
  const u16* xk = p.x + (size_t)(T0 + 16 * (j >> 1) + (lane >> 3)) * DM + 64 * w + 8 * (lane & 7);
  const u32x4 rxk0 = gld<u32x4>(xk), rxk1 = gld<u32x4>(xk + 8 * DM);
  asm volatile("s_barrier" ::: "memory");
  u32x4 rx[8], rwq[8];
  {
    const u16* xq = p.x + (size_t)(T0 + 16 * tq + h) * DM + 256 * kh + 8 * r32;
#pragma unroll
    for (int it = 0; it < 8; ++it) rx[it] = gld<u32x4>(xq + (size_t)it * 2 * DM);
  }
  lptr priv = lds + L_WQ + w * 2048;
  {
    lds_st<u32x4>(priv + lane * 16, rkv0);
    lds_st<u32x4>(priv + 1024 + lane * 16, rkv1);
    lds_st<u32x4>(priv + WSL + (lane >> 3) * 128 + (((lane & 7) ^ (lane >> 4)) << 4), rxk0);
    lds_st<u32x4>(priv + WSL + 1024 + (lane >> 3) * 128 + (((lane & 7) ^ (4 + (lane >> 4))) << 4), rxk1);
    lptr b = priv + (8 * g + q4) * 32 + 8 * pp;
    const bf16x8 f0 = trfrag(b, b + 128), f1 = trfrag(b + 1024, b + 1152);
    const bf16x8 xk0 = lds_ld<bf16x8>(priv + WSL + l16 * 128 + ((g ^ (l16 >> 1)) << 4));
    const bf16x8 xk1 = lds_ld<bf16x8>(priv + WSL + l16 * 128 + (((4 + g) ^ (l16 >> 1)) << 4));
    f32x4 acc = {0.f, 0.f, 0.f, 0.f};
    if (j & 1) { acc = mfma16(xk0, f0, acc); acc = mfma16(xk1, f1, acc); }
    else { acc = mfma16(f0, xk0, acc); acc = mfma16(f1, xk1, acc); }
    lds_st<f32x4>(lds + L_KVP + w * 1024 + lane * 16, acc);
    cnt_add(lds, C_KV);
  }
  STAMP(1);
  {
    const u16* src = p.wq + (size_t)(64 * w + (lane >> 3)) * DM + 64 * j + 8 * (lane & 7);
#pragma unroll
    for (int it = 0; it < 8; ++it) rwq[it] = gld<u32x4>(src + (size_t)it * 8 * DM);
  }
  u32x32 xf;
  {
#pragma unroll
    for (int it = 0; it < 8; ++it) {
      const int row = 2 * it + h, rot = 2 * (row & 7) - 2 * (row >> 2);
      lds_st<u32x4>(priv + (it >> 1) * WSL + (row & 3) * 512 + (r32 & 16) * 16 + (((r32 + rot) & 15) << 4), rx[it]);
    }
    const int rot = 2 * (l16 & 7) - 2 * (l16 >> 2);
    lptr rb = priv + (l16 >> 2) * WSL + (l16 & 3) * 512;
#pragma unroll
    for (int s2 = 0; s2 < 8; ++s2) {
      const u32x4 v = lds_ld<u32x4>(rb + (s2 >> 2) * 256 + (((4 * s2 + g + rot) & 15) << 4));
      xf[4 * s2] = v[0]; xf[4 * s2 + 1] = v[1]; xf[4 * s2 + 2] = v[2]; xf[4 * s2 + 3] = v[3];
    }
  }
  STAMP(4);
  {
    lptr st = priv + (((lane >> 1) & 3) ^ (2 * kh)) * WSL + (lane & 1) * 16;
#pragma unroll
    for (int it = 0; it < 8; ++it) lds_st<u32x4>(st + it * 256 + (((lane >> 3) ^ ((it & 1) << 2)) * 32), rwq[it]);
    cnt_add(lds, C_WQ + 4 * kh);
  }
  STAMP(2);
  const rsrc_t rs = ws_rsrc(p.ws);
  const u32 kp = WS_KF + w * 8192, vp = WS_VF + w * 8192;
  {
    const u32 lo16 = lane * 16, vo16 = (h * 16 + (r32 & 15)) * 16;
    const u32 lb = (u32)(uintptr_t)lds;
    const u32 qown = lb + L_EX + (((j >> 1) + 4 * (r32 >> 4)) ^ 4) * 2048 + (j & 1) * 512 + h * 256 + (r32 & 15) * 16;
    const u32 trb = lb + L_WQ + kh * 8192 + g * 256 + q4 * 32 + 8 * pp;
    const u32 trlo = trb + ((g & 1) << 7), trhi = trb + (((g & 1) ^ 1) << 7);
    const u32 qw = lb + L_EX + (w ^ 4) * 2048 + (l16 >> 3) * 512 + (g >> 1) * 256 + (l16 & 7) * 16 + 8 * (g & 1);
    const u32 pa = lb + L_PART + w * PW + h * 640 + r32 * 16;
    const u32 la = lb + (h ? L_DUMMY : L_L) + r32 * 32 + w * 4;
    const u64 vm = 0x0000ffff0000ffffull;
    u32 t0, t1, t2, t3, cnt = 4;
    asm volatile(
      "s_nop 4\n" AST(0)
      POLL
      "v_and_b32 v147, 0x1f0, %[lo16]\n v_mov_b32 v148, 0x3f803f80\n v_cmp_eq_u32 vcc, 0x100, v147\n"
      "v_mov_b32 v159, %[cwq]\n s_nop 1\n v_cndmask_b32 v147, 0, v148, vcc\n"
      FILL16(0) FILL16(16) FILL16(32) FILL16(48)
      SPIN("1", "v159", 0, 4) AST(1)
      "s_waitcnt vmcnt(0)\n v_cmp_ne_u64 vcc, %[tag], v[144:145]\n s_mov_b32 %[t1], 0\n s_cbranch_vccnz 2f\n"
      LOADKV
      "s_mov_b32 %[t1], 1\n"
      "2:\n" AST(2)
      TRD(0, 0) TRD(1, 0) TRD(2, 0) TRD(3, 0)
      GSTEP0 GSTEP(1, 2) GSTEP(2, 3) GSTEP(3, 4) GSTEP(4, 5) GSTEP(5, 6) GSTEP6 GSTEP7 AST(3)
      "v_add_u32 v144, %[exw], %[lo16]\n v_add_u32 v145, %[exr], %[lo16]\n v_mov_b32 v146, %[cex]\n v_mov_b32 v147, 1\n"
      "v_mov_b32 v159, %[cexp]\n"
      "ds_write_b128 v144, v[8:11]\n ds_write_b128 v144, v[12:15] offset:1024\n ds_write_b32 v146, v147\n"
      SPIN("3", "v159", 0, 1)
      "ds_read_b128 v[8:11], v145\n ds_read_b128 v[12:15], v145 offset:1024\n"
      "v_mov_b32 v146, %[cq]\n v_mov_b32 v159, %[cqo]\n"
      "s_waitcnt lgkmcnt(1)\n v_add_f32 v0, v0, v8\n v_add_f32 v1, v1, v9\n v_add_f32 v2, v2, v10\n v_add_f32 v3, v3, v11\n"
      "s_waitcnt lgkmcnt(0)\n v_add_f32 v4, v4, v12\n v_add_f32 v5, v5, v13\n v_add_f32 v6, v6, v14\n v_add_f32 v7, v7, v15\n"
      "v_cvt_pk_bf16_f32 v0, v0, v1\n v_cvt_pk_bf16_f32 v1, v2, v3\n v_cvt_pk_bf16_f32 v2, v4, v5\n v_cvt_pk_bf16_f32 v3, v6, v7\n"
      "ds_write_b64 %[qw], v[0:1]\n ds_write_b64 %[qw], v[2:3] offset:128\n"
      "s_mov_b64 exec, 1\n ds_add_u32 v146, v147\n s_mov_b64 exec, -1\n"
      "s_cmp_lg_u32 %[t1], 0\n s_cbranch_scc1 5f\n"
      "s_mov_b32 %[t1], 0x200000\n"
      "4:\n s_sleep 4\n"
      POLL
      "s_sub_u32 %[t1], %[t1], 1\n s_cmp_eq_u32 %[t1], 0\n s_cbranch_scc1 6f\n"
      "s_waitcnt vmcnt(0)\n v_cmp_ne_u64 vcc, %[tag], v[144:145]\n s_cbranch_vccnz 4b\n"
      "6:\n s_waitcnt vmcnt(0)\n"
      LOADKV
      "5:\n" AST(4)
      SPIN("7", "v159", 0, 2)
      "ds_read_b128 v[144:147], %[qown]\n"
      "v_mov_b32 v152, 0\n v_mov_b32 v153, 0\n v_mov_b32 v154, 0\n v_mov_b32 v155, 0\n v_mov_b32 v156, 0xdddd\n"
      "v_add_u32 v157, %[qr], %[lo16]\n"
      "s_waitcnt vmcnt(23) lgkmcnt(0)\n"
      AST(5) QK(16, 0, 144) ZO(144)
      "s_nop 9\n"
      ROUND0 AST(6)
      "8:\n"
      ROUND(148, 144, 0, 0, 2048)
      "s_sub_u32 %[cnt], %[cnt], 1\n s_cmp_eq_u32 %[cnt], 0\n s_cbranch_scc1 9f\n"
      ROUND(144, 148, 20480, 1024, 3072)
      "v_add_u32 v157, 0x800, v157\n s_branch 8b\n"
      "9:\n"
      PV(15, 40)
      "s_nop 11\n"
      PUT(20480, 1024)
      BAR
      : [cnt] "+s"(cnt), [t0] "=&s"(t0), [t1] "=&s"(t1), [t2] "=&s"(t2), [t3] "=&s"(t3), "+{v[16:47]}"(xf)
      : [lo16] "v"(lo16), [vo16] "v"(vo16), [pa] "v"(pa), [la] "v"(la), [trlo] "v"(trlo), [trhi] "v"(trhi), [qw] "v"(qw), [qown] "v"(qown),
        [rs] "s"(rs), [k0] "s"(kp), [k1] "s"(kp + 4096), [v0] "s"(vp), [v1] "s"(vp + 4096), [vm] "s"(vm), [tag] "s"(tag),
        [f1] "s"(WS_F1 + w * 32 * FS), [cwq] "s"(lb + L_CTL + C_WQ + 4 * kh), [exw] "s"(lb + L_EX + w * 2048), [exr] "s"(lb + L_EX + (w ^ 4) * 2048),
        [cex] "s"(lb + L_CTL + C_EX + 4 * w), [cexp] "s"(lb + L_CTL + C_EX + 4 * (w ^ 4)), [cq] "s"(lb + L_CTL + C_Q + 4 * tq),
        [cqo] "s"(lb + L_CTL + C_Q + 4 * (j >> 1)), [qr] "s"(lb + L_QR)
      : "memory", "scc", "vcc", CL10(), "v10", "v11", "v12", "v13", "v14", "v15", "v48", "v49", CL10(5), CL10(6), CL10(7), CL10(8), CL10(9),
        CL10(10), CL10(11), CL10(12), CL10(13), CL10(14), CL10(15) ASTCL);
    ASTOUT(0); ASTOUT(1); ASTOUT(2); ASTOUT(3); ASTOUT(4); ASTOUT(5); ASTOUT(6);
  }
  STAMP(3);
  CSTAMP(31);
  STAMP_DUMP();
  blk_barrier();
  if (__builtin_amdgcn_readfirstlane(lds_ld<u32>(lds + L_CTL + C_BAD)) != 0u) {
    const f32x16 zero16 = {0.f, 0.f, 0.f, 0.f, 0.f, 0.f, 0.f, 0.f, 0.f, 0.f, 0.f, 0.f, 0.f, 0.f, 0.f, 0.f};
    u32 ln;
    asm volatile("v_mbcnt_lo_u32_b32 %0, -1, 0\n\tv_mbcnt_hi_u32_b32 %0, -1, %0" : "=v"(ln));
    const u32 rr = ln & 31, hh = ln >> 5;
    const u32 lo16 = ln * 16, vo16 = (hh * 16 + (rr & 15)) * 16;
    const u32 qown = L_EX + (((j >> 1) + 4 * (rr >> 4)) ^ 4) * 2048 + (j & 1) * 512 + hh * 256 + (rr & 15) * 16;
    const u32 fill = (rr == 16) ? 0x3f803f80u : 0u;
    const u32x4 fill4 = {fill, fill, fill, fill};
#pragma unroll 1
    for (int r = 0; r < 8; ++r) {
      const bf16x8 qf = lds_ld<bf16x8>(lds + (r ? L_QR + r * 1024 + lo16 : qown));
      float mx = -INFINITY;
#pragma unroll 1
      for (int t = 0; t < 8; ++t) {
        f32x16 s = mfma32(bld8(rs, lo16, kp + t * 1024), qf, zero16);
#pragma unroll
        for (int e = 0; e < 16; ++e) mx = fmaxf(mx, s[e]);
      }
      { auto sw2 = __builtin_amdgcn_permlane32_swap(__float_as_uint(mx), __float_as_uint(mx), false, false);
        mx = fmaxf(__uint_as_float(sw2[0]), __uint_as_float(sw2[1])); }
      if (hh == 0) lds_st<float>(lds + L_MX + rr * 32 + w * 4, mx);
      blk_barrier();
      f32x4 m0 = lds_ld<f32x4>(lds + L_MX + rr * 32), m1 = lds_ld<f32x4>(lds + L_MX + rr * 32 + 16);
      mx = fmaxf(fmaxf(fmaxf(m0[0], m0[1]), fmaxf(m0[2], m0[3])), fmaxf(fmaxf(m1[0], m1[1]), fmaxf(m1[2], m1[3])));
      f32x16 o = zero16;
#pragma unroll 1
      for (int t = 0; t < 8; ++t) {
        f32x16 s = mfma32(bld8(rs, lo16, kp + t * 1024), qf, zero16);
        u32x4 v0 = bld4(rs, vo16, vp + t * 1024), v1 = bld4(rs, vo16, vp + t * 1024 + 512);
        if (rr >= 16) { v0 = fill4; v1 = fill4; }
#pragma unroll
        for (int e = 0; e < 16; ++e) s[e] = __builtin_amdgcn_exp2f(s[e] - mx);
        o = pv_tile(o, __builtin_bit_cast(bf16x8, v0), __builtin_bit_cast(bf16x8, v1), s);
      }
      lptr pq = lds + L_PART + (r & 1) * PBUF + w * PW + hh * 640 + rr * 16;
      f32x4 a = {o[0], o[1], o[2], o[3]}, b = {o[4], o[5], o[6], o[7]};
      lds_st<f32x4>(pq, a);
      lds_st<f32x4>(pq + 1280, b);
      if (hh == 0) lds_st<float>(lds + L_L + (r & 1) * LBUF + rr * 32 + w * 4, o[8]);
      blk_barrier();
    }
  }
}

DI f32x2 cmul(f32x2 a, f32x2 b) {
  f32x2 r;
  r[0] = __builtin_fmaf(a[0], b[0], -(a[1] * b[1]));
  r[1] = __builtin_fmaf(a[0], b[1], a[1] * b[0]);
  return r;
}
DI f32x2 cexp_t(float lr, float fr, float t) {
  const float mag = __builtin_amdgcn_exp2f(lr * t);
  const float ph = __builtin_amdgcn_fractf(fr * t);
  f32x2 r = {mag * __builtin_amdgcn_cosf(ph), mag * __builtin_amdgcn_sinf(ph)};
  return r;
}
template <int N> DI float ror16(float v) { return __uint_as_float(__builtin_amdgcn_update_dpp(0u, __float_as_uint(v), 0x120 + N, 0xf, 0xf, false)); }
DI float row_sum(float v) { v += ror16<8>(v); v += ror16<4>(v); v += ror16<2>(v); v += ror16<1>(v); return v; }
DI float wave_max(float v) {
  v = fmaxf(v, ror16<8>(v)); v = fmaxf(v, ror16<4>(v)); v = fmaxf(v, ror16<2>(v)); v = fmaxf(v, ror16<1>(v));
  const u32 u = __float_as_uint(v);
  const u32 a = __builtin_amdgcn_readlane(u, 0), b = __builtin_amdgcn_readlane(u, 16), c = __builtin_amdgcn_readlane(u, 32), d = __builtin_amdgcn_readlane(u, 48);
  return __uint_as_float(max(max(a, b), max(c, d)));
}

template <int SET> DI void conv_part(lptr fp, lptr gp, int r, f32x4 (&acc)[4]) {
  constexpr int NB[4] = {SET ? 1 : 0, SET ? 2 : 3, SET ? 5 : 4, SET ? 6 : 7};
  constexpr int MX = NB[3];
  bf16x8 gf[MX + 1];
  lptr g0 = gp - 64 * r, f0 = fp - 64 * r;
#pragma unroll
  for (int mm = 0; mm <= MX; ++mm) gf[mm] = lds_ld<bf16x8>(g0 + 512 * mm);
#pragma unroll
  for (int e = 0; e <= MX; ++e) {
    lptr fa = f0 + 512 * (7 - e);
    u32x4 fv = {lds_ld<u32>(fa), lds_ld<u32>(fa + 4), lds_ld<u32>(fa + 8), lds_ld<u32>(fa + 12)};
    const bf16x8 ff = __builtin_bit_cast(bf16x8, fv);
#pragma unroll
    for (int a = 0; a < 4; ++a)
      if (NB[a] >= e) acc[a] = mfma16(ff, gf[NB[a] - e], acc[a]);
  }
}

DI void svc_sync(lptr lds, u32 target) { cnt_add(lds, C_SVC); cnt_wait(lds, C_SVC, target); }

DI void load_wo(rsrc_t rs, u32 lo16, u32 wp, int grp, u32x8 (&wf)[8]) {
#pragma unroll
  for (int q = 0; q < 8; ++q) {
    u32x4 a = bld4(rs, lo16, wp + grp * 65536 + q * 2048), b = bld4(rs, lo16, wp + grp * 65536 + q * 2048 + 1024);
    wf[q] = __builtin_shufflevector(a, b, 0, 1, 2, 3, 4, 5, 6, 7);
  }
}

DI void svc_round(lptr lds, int buf, int sw, int lane, u32 target, const u32x8 (&wf)[8], f32x4 (&oacc)[4], bool& bad) {
  const int tt = lane & 7, dp = lane >> 3;
  lptr pp = lds + L_PART + buf * PBUF + (dp >> 1) * 640 + (8 * sw + tt) * 16 + 8 * (dp & 1);
  f32x2 v[8];
#pragma unroll
  for (int w = 0; w < 8; ++w) v[w] = lds_ld<f32x2>(pp + w * PW);
  const f32x4 l0 = lds_ld<f32x4>(lds + L_L + buf * LBUF + (8 * sw + tt) * 32), l1 = lds_ld<f32x4>(lds + L_L + buf * LBUF + (8 * sw + tt) * 32 + 16);
  const f32x2 s = ((v[0] + v[1]) + (v[2] + v[3])) + ((v[4] + v[5]) + (v[6] + v[7]));
  const f32x4 l4 = l0 + l1;
  const float l = (l4[0] + l4[1]) + (l4[2] + l4[3]);
  const float rl = __builtin_amdgcn_rcpf(l);
  const float o0 = s[0] * rl, o1 = s[1] * rl;
  bad = bad || !(l > 8.7e-19f && l < 1.1e30f && fabsf(s[0]) < 3e38f && fabsf(s[1]) < 3e38f);
  lds_st<u32>(lds + L_O + tt * OROW + 32 * sw + 4 * dp, pkbf(o0, o1));
  svc_sync(lds, target);
  const int l16 = lane & 15, g = lane >> 4;
  const int idx = (lane & 1) ? 0xDDDD : 0x8888;
  lptr ap = lds + L_O + (l16 >> 1) * OROW + 16 * g;
  const bf16x8 a0 = lds_ld<bf16x8>(ap), a1 = lds_ld<bf16x8>(ap + 64);
#pragma unroll
  for (int nt = 0; nt < 4; ++nt) oacc[nt] = __builtin_amdgcn_smfmac_f32_16x16x64_bf16(a0, __builtin_bit_cast(bf16x16, wf[nt]), oacc[nt], idx, 0, 0);
#pragma unroll
  for (int nt = 0; nt < 4; ++nt) oacc[nt] = __builtin_amdgcn_smfmac_f32_16x16x64_bf16(a1, __builtin_bit_cast(bf16x16, wf[4 + nt]), oacc[nt], idx, 0, 0);
}

DI void store_out(const Params& p, int m, int sw, int lane, const f32x4 (&oacc)[4], const float (&pre)[16], float g0, float g1) {
  const int l16 = lane & 15, g = lane >> 4;
  u32x4 o0, o1;
#pragma unroll
  for (int nt = 0; nt < 4; ++nt) {
    o0[nt] = pkbf(__builtin_fmaf(g0, oacc[nt][0], pre[2 * nt]), __builtin_fmaf(g0, oacc[nt][1], pre[2 * nt + 1]));
    o1[nt] = pkbf(__builtin_fmaf(g1, oacc[nt][2], pre[8 + 2 * nt]), __builtin_fmaf(g1, oacc[nt][3], pre[8 + 2 * nt + 1]));
  }
  u16* dst = p.out + (size_t)(8 * m + 2 * g) * DM + 128 * sw + 8 * l16;
  gst<u32x4>(dst, o0);
  gst<u32x4>(dst + DM, o1);
}

DI void set_flag(const Params& p, u32 off, u64 tag) { __hip_atomic_store((GAS u64*)(p.ws + off), tag, RLX_AGENT); }

DI void svc_role(const Params& p, lptr lds, int m, int sw, int lane, u64 tag) {
  __builtin_amdgcn_s_setprio(2);
  STAMP_INIT(8 + sw);
  STAMP(0);
  const int i = m >> 3, j = m & 7, T0 = 64 * i;
  const int l16 = lane & 15, g = lane >> 4, q4 = l16 >> 2, pp = l16 & 3;
  const int cl = sw >> 1, ch = 2 * m + cl, n = 64 * (sw & 1) + lane;
  const rsrc_t rs = ws_rsrc(p.ws);
  const u32 lo16 = lane * 16;
  const u32 taglo = (u32)tag, taghi = (u32)(tag >> 32);
  const u16 r_lar = gld<u16>(p.logA + ch * NMODE + n), r_aim = gld<u16>(p.Aim + ch * NMODE + n);
  const u16 r_cr = gld<u16>(p.Cre + ch * NMODE + n), r_ci = gld<u16>(p.Cim + ch * NMODE + n);
  const u16 r_cr2 = gld<u16>(p.Cre + ch * NMODE + (n ^ 64)), r_ci2 = gld<u16>(p.Cim + ch * NMODE + (n ^ 64));
  const u16 r_ldt = gld<u16>(p.logdt + ch);
  if (sw == 1 && lane < 32) lds_st<u32>(lds + L_CTL + lane * 4, 0u);
  blk_barrier();
  STAMP(1);
  const u16* xs = p.x + (size_t)(T0 + (lane >> 1)) * DM + 64 * j + 16 * sw + 8 * (lane & 1);
  const u32x4 xs0 = gld<u32x4>(xs), xs1 = gld<u32x4>(xs + 32 * DM);
  const u16* xsrc = p.x + (size_t)(8 * m + 2 * g) * DM + 128 * sw + 8 * l16;
  const u32x4 gx0 = gld<u32x4>(xsrc), gx1 = gld<u32x4>(xsrc + DM), gw = gld<u32x4>(p.gatew + 128 * sw + 8 * l16);
  const int wu = m >> 1, whf = wu & 1, wkb = (wu >> 1) & 1, ws2 = (wu >> 2) & 3, wr = wu >> 4;
  const bool wo_lane = (sw == 3) && ((lane >> 5) == (m & 1));
  u32x4 wv4[4];
  if (wo_lane) {
    const u16* src = p.wo + (size_t)(64 * wr + 32 * wkb + 16 * whf + 4 * g) * DM + 128 * ws2 + 8 * l16;
#pragma unroll
    for (int e = 0; e < 4; ++e) wv4[e] = gld<u32x4>(src + e * DM);
  }
  lptr img = lds + L_XY + cl * XYCH;
  if (sw == 0) {
    cnt_wait(lds, C_KV, 8u);
    f32x4 a[8];
#pragma unroll
    for (int w = 0; w < 8; ++w) a[w] = lds_ld<f32x4>(lds + L_KVP + w * 1024 + (((lane >> 1) & 15) + 16 * (2 * (lane >> 5) + (lane & 1))) * 16);
    f32x4 s = ((a[0] + a[1]) + (a[2] + a[3])) + ((a[4] + a[5]) + (a[6] + a[7]));
    const int tb = T0 + 16 * (j >> 1);
    u32 off;
    if (j & 1) off = WS_VF + (tb >> 4) * 512 + lane * 8;
    else { s *= 0.25f * 1.4426950408889634f; off = WS_KF + (tb >> 5) * 1024 + (tb & 31) * 16 + (lane >> 5) * 512 + (lane & 31) * 8; }
    u32x2 o = {pkbf(s[0], s[1]), pkbf(s[2], s[3])};
    bst2(rs, off, 0, o);
    STAMP(2);
  }
  {
    float s0 = 0.f, s1 = 0.f;
#pragma unroll
    for (int e = 0; e < 4; ++e) {
      const u32 p0 = pkbf(bflo(gx0[e]) * bflo(gw[e]), bfhi(gx0[e]) * bfhi(gw[e]));
      const u32 p1 = pkbf(bflo(gx1[e]) * bflo(gw[e]), bfhi(gx1[e]) * bfhi(gw[e]));
      s0 += bflo(p0) + bfhi(p0);
      s1 += bflo(p1) + bfhi(p1);
    }
    s0 = row_sum(s0); s1 = row_sum(s1);
    if (l16 == 0) { lds_st<float>(lds + L_G + (2 * g) * 16 + sw * 4, s0); lds_st<float>(lds + L_G + (2 * g + 1) * 16 + sw * 4, s1); }
  }
  if (sw == 0) {
    drain_vm();
    if (lane == 0) set_flag(p, WS_F1 + m * FS, tag);
    STAMP(3);
  }
  STAMP(4);
  u32 kk[8];
  {
    const float cr = bf2f(r_cr), ci = bf2f(r_ci);
    const float dt = bfr(expf(bf2f(r_ldt)));
    const float zr = -bfr(expf(bf2f(r_lar))) * dt, zi = bf2f(r_aim) * dt;
    const float lr = zr * 1.4426950408889634f, fr = zi * 0.15915494309189535f;
    float s = wave_max(fmaxf(fmaxf(fabsf(cr), fabsf(ci)), fmaxf(fabsf(bf2f(r_cr2)), fabsf(bf2f(r_ci2)))));
    s = fmaxf(s, 1e-30f);
    const float inv = 16384.f / s;
    const f32x2 c0 = {cr * inv, -ci * inv};
    const f32x2 e1 = cexp_t(lr, -fr, 1.f), e16 = cexp_t(lr, fr, 16.f), e256 = cexp_t(lr, -fr, 256.f);
    const f32x2 e2 = cmul(e1, e1), e4 = cmul(e2, e2), e8 = cmul(e4, e4);
    lptr wp = img + n * 4;
  {
    lptr st = img + (sw & 1) * 256;
    lds_st<u32x4>(st + (lane >> 4) * XROW + (lane & 15) * 16, xs0);
    lds_st<u32x4>(st + (4 + (lane >> 4)) * XROW + (lane & 15) * 16, xs1);
    lptr b = st + g * XROW + q4 * 32 + 8 * pp;
    const bf16x8 v0 = trfrag(b, b + 128), v1 = trfrag(b + 4 * XROW, b + 4 * XROW + 128);
    lptr t2 = st + 8 * XROW;
    lds_st<bf16x8>(t2 + (l16 >> 1) * XROW + (l16 & 1) * 128 + 16 * g, v0);
    lds_st<bf16x8>(t2 + (l16 >> 1) * XROW + (l16 & 1) * 128 + 64 + 16 * g, v1);
    const u32x4 w0 = lds_ld<u32x4>(t2 + g * XROW + l16 * 16), w1 = lds_ld<u32x4>(t2 + (4 + g) * XROW + l16 * 16);
    const u32 off = WS_XT + ((64 * j + 16 * sw + (lane >> 3)) * SEQ + T0 + 8 * (lane & 7)) * 2;
    bst4(rs, off, 0, w0);
    bst4(rs, off, 8 * SEQ * 2, w1);
  }
  if (wo_lane) {
    const u32 off = WS_WOF + (((wr * 4 + ws2) * 2 + wkb) * 8 + whf) * 1024 + lo16;
#pragma unroll
    for (int nt = 0; nt < 4; ++nt) { u32x4 o = {wv4[0][nt], wv4[1][nt], wv4[2][nt], wv4[3][nt]}; bst4(rs, off + nt * 2048, 0, o); }
  }
    STAMP(5);
#define PUTR(row, v) lds_st<u32>(wp + (row) * XROW, pkh(v))
    f32x2 st[8];
    st[0] = c0;
    {
      f32x2 ea = c0, eb = cmul(c0, e8), ya = {16384.f, 0.f}, yb = cexp_t(lr, fr, 128.f) * 16384.f;
#pragma unroll
      for (int r = 0; r < 8; ++r) {
        PUTR(r, ea); PUTR(8 + r, eb); PUTR(47 + r, ya); PUTR(55 + r, yb);
        if (r < 7) { ea = cmul(ea, e1); eb = cmul(eb, e1); ya = cmul(ya, e16); yb = cmul(yb, e16); st[r + 1] = cmul(st[r], e256); }
        __builtin_amdgcn_sched_barrier(0);
      }
    }
    {
      f32x2 a = st[1], b = cmul(st[1], e8), c = st[2], d = st[3];
#pragma unroll
      for (int q = 0; q < 5; ++q) {
        if (q < 4) PUTR(16 + q, a);
        PUTR(20 + q, b); PUTR(25 + q, c); PUTR(30 + q, d);
        if (q < 4) { if (q < 3) a = cmul(a, e2); b = cmul(b, e2); c = cmul(c, e4); d = cmul(d, e4); }
        __builtin_amdgcn_sched_barrier(0);
      }
#pragma unroll
      for (int q = 0; q < 3; ++q) {
#pragma unroll
        for (int T = 4; T < 8; ++T) { PUTR(35 + 3 * (T - 4) + q, st[T]); if (q < 2) st[T] = cmul(st[T], e8); }
        __builtin_amdgcn_sched_barrier(0);
      }
    }
#undef PUTR
    STAMP(6);
  {
    cnt_wait(lds, C_Q + 4 * sw, 2u);
    const int r32 = lane & 31, h = lane >> 5;
    lptr qa = lds + L_EX + ((sw + 4 * (r32 >> 4)) ^ 4) * 2048 + h * 256 + (r32 & 15) * 16;
    const u32x4 q0 = lds_ld<u32x4>(qa), q1 = lds_ld<u32x4>(qa + 512);
    const int j0 = 2 * sw, j1 = 2 * sw + 1;
    bst4(rs, lo16, WS_QF + ((8 * i + j0) * 8 + ((j - j0) & 7)) * 1024, q0);
    bst4(rs, lo16, WS_QF + ((8 * i + j1) * 8 + ((j - j1) & 7)) * 1024, q1);
    STAMP(9);
  }
    drain_vm();
    svc_sync(lds, 4u);
    if (sw == 0 && lane == 0) set_flag(p, WS_F2 + m * FS, tag);
    STAMP(7);
    const float sc = (2.f * dt) * (s * (1.f / 268435456.f));
    const int rho = 15 - l16;
    lptr ap[4];
#pragma unroll
    for (int tt = 0; tt < 4; ++tt) {
      int xi;
      if (sw & 1) xi = 35 + 3 * tt + ((rho + 3 + ((rho >> 3) & 1)) >> 3);
      else if (tt == 0) xi = rho;
      else if (tt == 1) xi = 16 + ((rho + ((rho >> 1) & 1)) >> 1);
      else xi = 25 + 5 * (tt - 2) + ((rho + 1 + ((rho >> 2) & 1)) >> 2);
      ap[tt] = img + xi * XROW + 16 * g;
    }
    lptr yp = img + (47 + l16) * XROW + 16 * g;
    f32x4 acc[4];
#pragma unroll
    for (int tt = 0; tt < 4; ++tt) acc[tt] = f32x4{0.f, 0.f, 0.f, 0.f};
#pragma unroll
    for (int ks = 0; ks < 8; ++ks) {
      const f16x8 yb = lds_ld<f16x8>(yp + 64 * ks);
#pragma unroll
      for (int tt = 0; tt < 4; ++tt) acc[tt] = __builtin_amdgcn_mfma_f32_16x16x32_f16(lds_ld<f16x8>(ap[tt] + 64 * ks), yb, acc[tt], 0, 0, 0);
    }
#pragma unroll
    for (int tt = 0; tt < 4; ++tt) { kk[2 * tt] = pkbf(acc[tt][0] * sc, acc[tt][1] * sc); kk[2 * tt + 1] = pkbf(acc[tt][2] * sc, acc[tt][3] * sc); }
  }
  STAMP(8);
  svc_sync(lds, 8u);
  STAMP(10);
  lptr ub = lds + L_U + cl * UCH;
  {
    lptr r0 = lds + L_R + cl * (2 * RCOPY), r1 = r0 + RCOPY;
#pragma unroll
    for (int tt = 0; tt < 4; ++tt) {
      const int i0 = 2032 - 256 * (4 * (sw & 1) + tt) - 16 * l16 + 4 * g;
      const u32 a = kk[2 * tt], b = kk[2 * tt + 1];
      u32x2 ab = {a, b};
      lds_st<u32x2>(r0 + i0 * 2, ab);
      if (i0 > 0) lds_st<u16>(r1 + i0 * 2 - 2, (u16)a);
      lds_st<u32>(r1 + i0 * 2, (a >> 16) | (b << 16));
      lds_st<u16>(r1 + i0 * 2 + 4, (u16)(b >> 16));
    }
    if ((sw & 1) == 0) {
      u32x2 z = {0u, 0u};
      lds_st<u32x2>(ub + lane * 8, z);
      if (lane < 16) { lds_st<u32>(r0 + 4096 + lane * 4, 0u); lds_st<u32>(r1 + 4096 + lane * 4, 0u); }
      if (lane == 0) lds_st<u16>(r1 + 4094, (u16)0);
    }
  }
  if (sw == 0) {
#pragma unroll 1
    for (u32 spins = 0; spins < (1u << 21); ++spins) {
      const u32x2 f = bld2(rs, (lane & 7) * FS, WS_F2 + i * 8 * FS);
      if (__all(f[0] == taglo && f[1] == taghi)) break;
      __builtin_amdgcn_s_sleep(2);
    }
    __builtin_amdgcn_fence(__ATOMIC_ACQUIRE, "wavefront");
    STAMP(11);
    u32x4 qv[7];
#pragma unroll
    for (int r = 0; r < 7; ++r) qv[r] = bld4(rs, lo16, WS_QF + (m * 8 + 1 + r) * 1024);
#pragma unroll
    for (int r = 0; r < 7; ++r) lds_st<u32x4>(lds + L_QR + (1 + r) * 1024 + lo16, qv[r]);
  } else if (sw == 1) {
#pragma unroll 1
    for (u32 spins = 0; spins < (1u << 21); ++spins) {
      __builtin_amdgcn_s_sleep(8);
      const u32x2 f0 = bld2(rs, lane * FS, WS_F2), f1 = bld2(rs, lane * FS, WS_F2 + 64 * FS), f2 = bld2(rs, lane * FS, WS_F2 + 128 * FS), f3 = bld2(rs, lane * FS, WS_F2 + 192 * FS);
      const bool ok = f0[0] == taglo && f0[1] == taghi && f1[0] == taglo && f1[1] == taghi && f2[0] == taglo && f2[1] == taghi && f3[0] == taglo && f3[1] == taghi;
      if (__all(ok)) break;
    }
    STAMP(11);
  }
  const u32 wp = WS_WOF + sw * 16384;
  u32x8 wf[8];
  lptr fp = lds + L_R + cl * (2 * RCOPY) + ((l16 & 1) ? 0 : RCOPY) + (1015 - 16 * 56 - (l16 >> 1) + 4 * g) * 4;
  lptr gp = ub + 512 + 32 * l16 - 32 + 16 * g;
  f32x4 oacc[4], cacc[4];
#pragma unroll
  for (int a = 0; a < 4; ++a) { oacc[a] = f32x4{0.f, 0.f, 0.f, 0.f}; cacc[a] = oacc[a]; }
  bool bad = false;
  STAMP(12);
  blk_barrier();
  STAMP(13);
  {
    const u32x4 u0 = bld4(rs, lo16, WS_XT + ch * (SEQ * 2) + (sw & 1) * 2048), u1 = bld4(rs, lo16, WS_XT + ch * (SEQ * 2) + (sw & 1) * 2048 + 1024);
    load_wo(rs, lo16, wp, j, wf);
    lds_st<u32x4>(ub + 512 + (sw & 1) * 2048 + lo16, u0);
    lds_st<u32x4>(ub + 512 + (sw & 1) * 2048 + 1024 + lo16, u1);
  }
#pragma unroll 1
  for (int s = 1; s <= 4; ++s) {
    blk_barrier();
    STAMP(13 + s);
    svc_round(lds, (s - 1) & 1, sw, lane, 8u + 4u * s, wf, oacc, bad);
    if (sw & 1) { conv_part<1>(fp, gp, 2 * s - 2, cacc); conv_part<1>(fp, gp, 2 * s - 1, cacc); }
    else { conv_part<0>(fp, gp, 2 * s - 2, cacc); conv_part<0>(fp, gp, 2 * s - 1, cacc); }
    load_wo(rs, lo16, wp, (j + s) & 7, wf);
  }
  {
    lptr ys = lds + L_YS + sw * YSW;
#pragma unroll
    for (int a = 0; a < 4; ++a) {
      u32x2 v = {pkbf(cacc[a][0], cacc[a][1]), pkbf(cacc[a][2], cacc[a][3])};
      lds_st<u32x2>(ys + a * 512 + 32 * l16 + 8 * g, v);
    }
    const u32x4 y0 = lds_ld<u32x4>(ys + lo16), y1 = lds_ld<u32x4>(ys + 1024 + lo16);
    const int hh = lane >> 5;
    const int nb0 = (sw & 1) ? 1 + hh : 3 * hh, nb1 = nb0 + 4;
    const u32 yo = WS_YT + ch * (SEQ * 2) + (lane & 31) * 16;
    bst4(rs, yo + nb0 * 512, 0, y0);
    bst4(rs, yo + nb1 * 512, 0, y1);
  }
#pragma unroll 1
  for (int s = 5; s <= 7; ++s) {
    blk_barrier();
    STAMP(13 + s);
    drain_vm();
    svc_round(lds, (s - 1) & 1, sw, lane, 8u + 4u * s, wf, oacc, bad);
    if (s == 5 && sw == 0 && lane == 0) set_flag(p, WS_F3 + m * FS, tag);
    load_wo(rs, lo16, wp, (j + s) & 7, wf);
  }
  float pre[16], g0, g1;
  {
#pragma unroll 1
    for (u32 spins = 0; spins < (1u << 21); ++spins) {
      const u32x2 f = bld2(rs, lane * FS, WS_F3 + sw * 64 * FS);
      if (__all(f[0] == taglo && f[1] == taghi)) break;
      __builtin_amdgcn_s_sleep(1);
    }
    __builtin_amdgcn_fence(__ATOMIC_ACQUIRE, "wavefront");
    const u32 yo = (128 * sw + lane) * (SEQ * 2) + 16 * m;
    const u32x4 y0 = bld4(rs, yo, WS_YT), y1 = bld4(rs, yo, WS_YT + 64 * (SEQ * 2));
    const u32x4 x0 = gld<u32x4>(xsrc), x1 = gld<u32x4>(xsrc + DM), dk = gld<u32x4>(p.Dskip + 128 * sw + 8 * l16);
    lptr ys = lds + L_YS + sw * YSW;
    lds_st<u32x4>(ys + (lane >> 3) * 144 + (lane & 7) * 16, y0);
    lds_st<u32x4>(ys + (8 + (lane >> 3)) * 144 + (lane & 7) * 16, y1);
    u32 yv[8];
#pragma unroll
    for (int e = 0; e < 8; ++e) yv[e] = lds_ld<u32>(ys + l16 * 144 + e * 16 + 4 * g);
    const f32x4 ga = lds_ld<f32x4>(lds + L_G + (2 * g) * 16), gb = lds_ld<f32x4>(lds + L_G + (2 * g + 1) * 16);
    const float z0 = bfr((ga[0] + ga[1]) + (ga[2] + ga[3])), z1 = bfr((gb[0] + gb[1]) + (gb[2] + gb[3]));
    g0 = bfr(1.f / (1.f + __expf(-z0)));
    g1 = bfr(1.f / (1.f + __expf(-z1)));
    const float h0 = bfr(1.f - g0), h1 = bfr(1.f - g1);
#pragma unroll
    for (int e = 0; e < 4; ++e) {
      pre[2 * e] = h0 * __builtin_fmaf(bflo(x0[e]), bflo(dk[e]), bflo(yv[2 * e]));
      pre[2 * e + 1] = h0 * __builtin_fmaf(bfhi(x0[e]), bfhi(dk[e]), bflo(yv[2 * e + 1]));
      pre[8 + 2 * e] = h1 * __builtin_fmaf(bflo(x1[e]), bflo(dk[e]), bfhi(yv[2 * e]));
      pre[8 + 2 * e + 1] = h1 * __builtin_fmaf(bfhi(x1[e]), bfhi(dk[e]), bfhi(yv[2 * e + 1]));
    }
  }
  STAMP(21);
  blk_barrier();
  STAMP(22);
  svc_round(lds, 1, sw, lane, 40u, wf, oacc, bad);
  store_out(p, m, sw, lane, oacc, pre, g0, g1);
  STAMP(23);
  STAMP_DUMP();
  if (__any(bad) && lane == 0) lds_st<u32>(lds + L_CTL + C_BAD, 1u);
  if (m == 0 && sw == 0 && lane == 0) set_flag(p, WS_EP, tag);
  blk_barrier();
  if (__builtin_amdgcn_readfirstlane(lds_ld<u32>(lds + L_CTL + C_BAD)) != 0u) {
#pragma unroll
    for (int a = 0; a < 4; ++a) oacc[a] = f32x4{0.f, 0.f, 0.f, 0.f};
#pragma unroll 1
    for (int r = 0; r < 8; ++r) {
      load_wo(rs, lo16, wp, (j + r) & 7, wf);
      blk_barrier();
      blk_barrier();
      svc_round(lds, r & 1, sw, lane, 44u + 4u * r, wf, oacc, bad);
    }
    store_out(p, m, sw, lane, oacc, pre, g0, g1);
  }
}

__global__ __launch_bounds__(768) void k_fused(Params p) {
  __shared__ __attribute__((aligned(16))) char smem[L_BYTES + STAMP_BYTES];
  lptr lds = (lptr)smem;
  const int b = blockIdx.x, m = 32 * (b & 7) + (b >> 3);
  const int w = __builtin_amdgcn_readfirstlane(threadIdx.x >> 6), lane = threadIdx.x & 63;
  const u64 tag = *(const u64*)(p.ws + WS_EP) * 6364136223846793005ull + 1442695040888963407ull;
  if (w < 8) attn_role(p, lds, m, w, lane, tag);
  else svc_role(p, lds, m, w - 8, lane, tag);
}

extern "C" void kernel_launch(void* const* d_in, const int* in_sizes, int n_in, void* d_out, int out_size,
                              void* d_ws, size_t ws_size, hipStream_t stream) {
  Params p;
  p.x = (const u16*)d_in[0]; p.wq = (const u16*)d_in[1]; p.wk = (const u16*)d_in[2]; p.wv = (const u16*)d_in[3];
  p.wo = (const u16*)d_in[4]; p.logA = (const u16*)d_in[6]; p.Aim = (const u16*)d_in[7]; p.Cre = (const u16*)d_in[8];
  p.Cim = (const u16*)d_in[9]; p.logdt = (const u16*)d_in[10]; p.Dskip = (const u16*)d_in[11]; p.gatew = (const u16*)d_in[12];
  p.out = (u16*)d_out; p.ws = (char*)d_ws;
  k_fused<<<256, 768, 0, stream>>>(p);
}
```

```cpp
#include <hip/hip_runtime.h>
#include <stdint.h>

typedef unsigned short u16;
typedef unsigned int u32;
typedef unsigned long long u64;
typedef __bf16 bf16x2 __attribute__((ext_vector_type(2)));
typedef __bf16 bf16x8 __attribute__((ext_vector_type(8)));
typedef __bf16 bf16x16 __attribute__((ext_vector_type(16)));
typedef _Float16 f16x2 __attribute__((ext_vector_type(2)));
typedef _Float16 f16x8 __attribute__((ext_vector_type(8)));
typedef float f32x2 __attribute__((ext_vector_type(2)));
typedef float f32x4 __attribute__((ext_vector_type(4)));
typedef float f32x8 __attribute__((ext_vector_type(8)));
typedef float f32x16 __attribute__((ext_vector_type(16)));
typedef u32 u32x2 __attribute__((ext_vector_type(2)));
typedef u32 u32x4 __attribute__((ext_vector_type(4)));
typedef u32 u32x8 __attribute__((ext_vector_type(8)));
typedef short s16x4 __attribute__((ext_vector_type(4)));
#define LAS __attribute__((address_space(3)))
#define GAS __attribute__((address_space(1)))
#define DI __device__ __forceinline__
#define RLX_AGENT __ATOMIC_RELAXED, __HIP_MEMORY_SCOPE_AGENT
#define RLX_WG __ATOMIC_RELAXED, __HIP_MEMORY_SCOPE_WORKGROUP
typedef LAS char* lptr;

constexpr int SEQ = 2048, DM = 512, NMODE = 128;
constexpr u32 WS_EP = 0;
constexpr u32 WS_CNT = 4096;
constexpr u32 WS_GO = 16384;
constexpr u32 WS_KF = 131072;
constexpr u32 WS_VF = WS_KF + 65536;
constexpr u32 WS_WOF = WS_VF + 65536;
constexpr u32 WS_QF = WS_WOF + 524288;
constexpr u32 WS_QP = WS_QF + 1048576;
constexpr u32 WS_XT = WS_QP + 2097152;
constexpr u32 WS_YT = WS_XT + 2097152;
constexpr u32 WS_BYTES = WS_YT + 2097152;

#define TAG_A0 0x2c9277b5
#define TAG_C0 0xac564b05
#define TAG_A1 0x0019660d
#define TAG_C1 0x3c6ef35f
static_assert(WS_EP == 0, "read at offset 0 in the asm");

#define PROBE(n) do {} while (0)
#define KVWARM 1
#define GATEN 8
struct Params {
  char* ws;
  const u16 *x, *wq, *wk, *wv, *wo, *logA, *Aim, *Cre, *Cim, *logdt, *Dskip, *gatew;
  u16* out;
};

typedef u64 u64x8 __attribute__((ext_vector_type(8)));
typedef u64 u64x4 __attribute__((ext_vector_type(4)));
typedef u64 u64x2 __attribute__((ext_vector_type(2)));
struct Args { u64x8 a; u64x4 b; u64x2 c; };
__device__ __forceinline__ Params wait_params() {
  Args ka;
  asm volatile("s_waitcnt lgkmcnt(0)" : "={s[72:87]}"(ka.a), "={s[88:95]}"(ka.b), "={s[96:99]}"(ka.c) :: "memory");
  Params p;
  p.ws = (char*)ka.a[0]; p.x = (const u16*)ka.a[1]; p.wq = (const u16*)ka.a[2]; p.wk = (const u16*)ka.a[3]; p.wv = (const u16*)ka.a[4];
  p.wo = (const u16*)ka.a[5]; p.logA = (const u16*)ka.a[6]; p.Aim = (const u16*)ka.a[7]; p.Cre = (const u16*)ka.b[0];
  p.Cim = (const u16*)ka.b[1]; p.logdt = (const u16*)ka.b[2]; p.Dskip = (const u16*)ka.b[3]; p.gatew = (const u16*)ka.c[0]; p.out = (u16*)ka.c[1];
  return p;
}

#define CSTAMP(id) do {} while (0)
#define STAMP(id) do {} while (0)
#define STAMP_INIT(w_) do {} while (0)
#define STAMP_DUMP() do {} while (0)
constexpr int STAMP_BYTES = 0;
DI float bf2f(u16 v) { return __uint_as_float((u32)v << 16); }
DI float bflo(u32 v) { return __uint_as_float(v << 16); }
DI float bfhi(u32 v) { return __uint_as_float(v & 0xffff0000u); }
DI u32 pkbf(float a, float b) { f32x2 v = {a, b}; return __builtin_bit_cast(u32, __builtin_convertvector(v, bf16x2)); }
DI float bfr(float a) { return bflo(pkbf(a, a)); }
DI u32 pkh(f32x2 v) { return __builtin_bit_cast(u32, __builtin_convertvector(v, f16x2)); }
DI f32x4 mfma16(bf16x8 a, bf16x8 b, f32x4 c) { return __builtin_amdgcn_mfma_f32_16x16x32_bf16(a, b, c, 0, 0, 0); }
DI f32x16 mfma32(bf16x8 a, bf16x8 b, f32x16 c) { return __builtin_amdgcn_mfma_f32_32x32x16_bf16(a, b, c, 0, 0, 0); }
template <class T> DI T lds_ld(lptr p) { return *(LAS T*)p; }
template <class T> DI void lds_st(lptr p, T v) { *(LAS T*)p = v; }
template <class T> DI T gld(const void* p) { return *(const GAS T*)p; }
template <class T> DI void gst(void* p, T v) { *(GAS T*)p = v; }
typedef __amdgpu_buffer_rsrc_t rsrc_t;
DI rsrc_t ws_rsrc(char* ws) { return __builtin_amdgcn_make_buffer_rsrc(ws, 0, WS_BYTES, 0x00020000); }
DI u32x4 bld4(rsrc_t r, u32 voff, u32 soff) { return __builtin_amdgcn_raw_buffer_load_b128(r, voff, soff, 16); }
DI u32x2 bld2(rsrc_t r, u32 voff, u32 soff) { return __builtin_amdgcn_raw_buffer_load_b64(r, voff, soff, 16); }
DI u32 bld1(rsrc_t r, u32 voff, u32 soff) { return __builtin_amdgcn_raw_buffer_load_b32(r, voff, soff, 16); }
DI bf16x8 bld8(rsrc_t r, u32 voff, u32 soff) { return __builtin_bit_cast(bf16x8, bld4(r, voff, soff)); }
DI void bst4(rsrc_t r, u32 voff, u32 soff, u32x4 v) { __builtin_amdgcn_raw_buffer_store_b128(v, r, voff, soff, 16); }
DI void bst2(rsrc_t r, u32 voff, u32 soff, u32x2 v) { __builtin_amdgcn_raw_buffer_store_b64(v, r, voff, soff, 16); }
DI bf16x8 trfrag(lptr lo_p, lptr hi_p) {
  s16x4 lo = __builtin_amdgcn_ds_read_tr16_b64_v4i16((LAS s16x4*)lo_p);
  s16x4 hi = __builtin_amdgcn_ds_read_tr16_b64_v4i16((LAS s16x4*)hi_p);
  return __builtin_bit_cast(bf16x8, __builtin_shufflevector(lo, hi, 0, 1, 2, 3, 4, 5, 6, 7));
}
DI u32 lane_id() {
  u32 ln;
  asm volatile("v_mbcnt_lo_u32_b32 %0, -1, 0\n\tv_mbcnt_hi_u32_b32 %0, -1, %0" : "=v"(ln));
  __builtin_assume(ln < 64u);
  return ln;
}
DI void drain_vm() { asm volatile("s_waitcnt vmcnt(0)" ::: "memory"); }
DI void blk_barrier() { asm volatile("s_waitcnt lgkmcnt(0)\n\ts_barrier" ::: "memory"); }

constexpr int L_WQ = 0;
constexpr int L_KVP = 65536;
constexpr int L_EX = L_KVP + 8192;
constexpr int XROW = 528, XYCH = 63 * XROW;
constexpr int L_XY = L_EX + 16384;
constexpr int L_G = L_XY + 2 * XYCH;
constexpr int L_CTL = L_G + 128;
constexpr int L_TAB = L_CTL + 128;
constexpr int L_BYTES = L_TAB + 3072;
constexpr int PW = 2048;
constexpr int L_PART = 0, PBUF = 8 * PW;
constexpr int L_L = L_EX + 8192, LBUF = 2048;
constexpr int L_O = L_EX + 4096, OROW = 144;
constexpr int L_MX = L_KVP;
constexpr int RCOPY = 4160, CH_U = 4 * RCOPY, CH_YS = CH_U + 4608, YSW = 4096;
static_assert(CH_YS + 2 * YSW <= XYCH && L_XY % 1024 == 0 && 2 * XYCH >= 65536, "channel images; the x image lives here first");
constexpr int L_QR = L_KVP;
constexpr int C_SVC = 0, C_BAD = 4, C_KV = 8, C_WQ = 16  , C_XT = 24, C_F2 = 12, C_XY = 48  , C_KG = 56  , C_Q = 32  , C_EX = 64  , C_P = 96  ;

DI void cnt_add(lptr lds, int off) {
  asm volatile("s_mov_b64 exec, 1\n\tds_add_u32 %0, %1\n\ts_mov_b64 exec, -1" :: "v"((u32)(uintptr_t)(lds + L_CTL + off)), "v"(1u) : "memory");
}
template <bool SLEEP = true> DI void cnt_wait(lptr lds, int off, u32 target) {
#pragma unroll 1
  for (u32 spins = 0; spins < (1u << 22); ++spins) {
    if ((u32)__builtin_amdgcn_readfirstlane(__hip_atomic_load((LAS u32*)(lds + L_CTL + off), RLX_WG)) >= target) break;
    if (SLEEP) __builtin_amdgcn_s_sleep(1);
  }
  asm volatile("" ::: "memory");
}

DI f32x16 pv_tile(f32x16 o, bf16x8 v0, bf16x8 v1, const f32x16& pr, int idx) {
  f32x8 lo = __builtin_shufflevector(pr, pr, 0, 1, 2, 3, 4, 5, 6, 7);
  f32x8 hi = __builtin_shufflevector(pr, pr, 8, 9, 10, 11, 12, 13, 14, 15);
  o = __builtin_amdgcn_smfmac_f32_32x32x32_bf16(v0, __builtin_bit_cast(bf16x16, lo), o, idx, 0, 0);
  o = __builtin_amdgcn_smfmac_f32_32x32x32_bf16(v1, __builtin_bit_cast(bf16x16, hi), o, idx, 0, 0);
  return o;
}

#define ST_(x) #x
#define ST(x) ST_(x)
#define EX1(b, i) "v_exp_f32 v[" ST(b) "+" ST(i) "], v[" ST(b) "+" ST(i) "]\n"
#define EX_0_2(b) EX1(b, 0) EX1(b, 1)
#define EX_2_5(b) EX1(b, 2) EX1(b, 3) EX1(b, 4)
#define EX_5_10(b) EX1(b, 5) EX1(b, 6) EX1(b, 7) EX1(b, 8) EX1(b, 9)
#define EX_10_13(b) EX1(b, 10) EX1(b, 11) EX1(b, 12)
#define EX_13_16(b) EX1(b, 13) EX1(b, 14) EX1(b, 15)
#define PV(vi, pb) "v_smfmac_f32_32x32x32_bf16 v[0:15], v[80+4*" ST(vi) ":83+4*" ST(vi) "], v[" ST(pb) ":" ST(pb) "+7], v156\n"
#define QK(d, kt, q) "v_mfma_f32_32x32x16_bf16 v[" ST(d) ":" ST(d) "+15], v[48+4*" ST(kt) ":51+4*" ST(kt) "], v[" ST(q) ":" ST(q) "+3], 0\n"
#define ZO(q) "v_mfma_f32_32x32x16_bf16 v[0:15], v[152:155], v[" ST(q) ":" ST(q) "+3], 0\n"
#define TILE(W, C, N, vprev, vcur, kn, q) W EX_0_2(C) PV(vprev, N+8) EX_2_5(C) QK(N, kn, q) EX_5_10(C) PV(vcur, C) EX_10_13(C) EX_13_16(C)
#define PUT(po, lo, co) "ds_write_b128 v165, v[0:3] offset:" ST(po) "\n ds_write_b128 v165, v[4:7] offset:" ST(po) "+1024\n ds_write_b32 v166, v8 offset:" ST(lo) "\n" \
  "s_mov_b64 exec, 1\n ds_add_u32 v162, v163 offset:" ST(co) "\n s_mov_b64 exec, -1\n"
#define BAR "s_waitcnt lgkmcnt(0)\n s_barrier\n"
#define LG0 "s_waitcnt lgkmcnt(0)\n"
#define RDQ(qn, off) "ds_read_b128 v[" ST(qn) ":" ST(qn) "+3], v157 offset:" ST(off) "\n"
#define PRIO(br, l) "s_cmp_lt_u32 %[wv], 4\n " br " " l "3f\n s_cmp_eq_u32 s60, 3\n s_cbranch_scc1 " l "3f\n s_cmp_eq_u32 s60, 2\n s_cbranch_scc1 " l "1f\n s_cmp_eq_u32 s60, 1\n s_cbranch_scc1 " l "2f\n" \
  "s_setprio 0\n s_branch " l "3f\n" l "1:\n s_setprio 2\n s_branch " l "3f\n" l "2:\n s_setprio 1\n" l "3:\n"
#define CHK(l) LG0 "v_readfirstlane_b32 s56, v158\n s_max_i32 s57, s61, 4\n s_add_u32 s61, s61, 4\n s_cmp_ge_u32 s56, s57\n s_cbranch_scc1 " l "2f\n" \
  "s_mov_b32 s58, 0\n" l "1:\n s_sleep 1\n ds_read_b32 v158, v159\n s_add_u32 s58, s58, 0x400\n s_cselect_b32 s59, -1, 0\n" \
  "s_waitcnt lgkmcnt(0)\n v_readfirstlane_b32 s56, v158\n s_or_b32 s56, s56, s59\n s_cmp_lt_u32 s56, s57\n s_cbranch_scc1 " l "1b\n" l "2:\n"
#define ROUND(q, qn, po, lo, co, X3, X5, X7) \
  EX_0_2(16) PV(15, 40) EX_2_5(16) QK(32, 1, q) EX_5_10(16) EX_10_13(16) PUT(po, lo, co) ZO(q) EX_13_16(16) PV(0, 16) \
  TILE("", 32, 16, 1, 2, 2, q) TILE("", 16, 32, 3, 4, 3, q) TILE(X3, 32, 16, 5, 6, 4, q) \
  TILE("ds_read_b32 v158, v159\n", 16, 32, 7, 8, 5, q) TILE(X5, 32, 16, 9, 10, 6, q) TILE("", 16, 32, 11, 12, 7, q) \
  TILE(X7, 32, 16, 13, 14, 0, qn)
#define IF1(l) "s_cmp_lg_u32 s60, 3\n s_cbranch_scc1 " l "f\n"
#define VM(n) "s_waitcnt vmcnt(" ST(n) ")\n"
#define ROUND0 \
  VM(7) LG_0 EX_0_2(16) EX_2_5(16) QK(32, 1, 144) EX_5_10(16) VM(8) PV(0, 16) EX_10_13(16) EX_13_16(16) \
  TILE(VM(6) LG_1, 32, 16, 1, 2, 2, 144) TILE(VM(6) LG_2, 16, 32, 3, 4, 3, 144) TILE(VM(6) LG_3, 32, 16, 5, 6, 4, 144) \
  TILE(VM(6) LG_4, 16, 32, 7, 8, 5, 144) TILE(VM(6) LDQ(148, 0), 32, 16, 9, 10, 6, 144) \
  TILE(VM(5), 16, 32, 11, 12, 7, 144) TILE(AST(8) QCHK(148, 0, "3"), 32, 16, 13, 14, 0, 148)
#define LDQ(d, o) "buffer_load_dwordx4 v[" ST(d) ":" ST(d) "+3], v167, %[rs], s43 offen offset:" ST(o) " sc1\n" \
  "buffer_load_dwordx4 v[152:155], v167, %[rs], s43 offen offset:" ST(o) "+128 sc1\n"
#define QCHK(d, o, l) "s_mov_b32 s57, 0x200000\n" l "0:\n" VM(0) \
  "v_cmp_ne_u32 vcc, s52, v[" ST(d) "+1]\n v_cmp_ne_u32 s[58:59], s52, v[" ST(d) "+3]\n s_or_b64 s[58:59], s[58:59], vcc\n" \
  "v_cmp_ne_u32 vcc, s52, v153\n s_or_b64 s[58:59], s[58:59], vcc\n v_cmp_ne_u32 vcc, s52, v155\n s_or_b64 s[58:59], s[58:59], vcc\n" \
  "s_cbranch_scc0 " l "1f\n s_sub_u32 s57, s57, 1\n s_cbranch_scc1 " l "1f\n s_sleep 8\n" LDQ(d, o) "s_branch " l "0b\n" l "1:\n" \
  "v_mov_b32 v[" ST(d) "+1], v[" ST(d) "+2]\n v_mov_b32 v[" ST(d) "+2], v152\n v_mov_b32 v[" ST(d) "+3], v154\n" \
  "v_mov_b64 v[152:153], 0\n v_mov_b64 v[154:155], 0\n"
#define LDK(t, so, off) "buffer_load_dwordx4 v[48+4*" ST(t) ":51+4*" ST(t) "], v160, %[rs], " so " offen offset:" ST(off) " sc1\n"
#define LDV(i, so, off) "buffer_load_dwordx4 v[80+4*" ST(i) ":83+4*" ST(i) "], v161, %[rs], " so " offen offset:" ST(off) " sc1\n"
#define XV "s_mov_b64 exec, s[36:37]\n"
#define XA "s_mov_b64 exec, -1\n"
#define LOADKV \
  LDK(0, "s41", 0) LDK(1, "s41", 1024) LDK(2, "s41", 2048) XV LDV(0, "s39", 0) LDV(1, "s39", 512) LDV(2, "s39", 1024) XA \
  LDK(3, "s41", 3072) XV LDV(3, "s39", 1536) LDV(4, "s39", 2048) XA
#define LG_0 LDK(4, "s38", 0) XV LDV(5, "s39", 2560) LDV(6, "s39", 3072) XA
#define LG_1 LDK(5, "s38", 1024) XV LDV(7, "s39", 3584) LDV(8, "s40", 0) XA
#define LG_2 LDK(6, "s38", 2048) XV LDV(9, "s40", 512) LDV(10, "s40", 1024) XA
#define LG_3 LDK(7, "s38", 3072) XV LDV(11, "s40", 1536) LDV(12, "s40", 2048) XA
#define LG_4 XV LDV(13, "s40", 2560) LDV(14, "s40", 3072) LDV(15, "s40", 3584) XA
#define POLL "buffer_load_dwordx2 v[150:151], off, %[rs], s42 sc1\n"
#define FILLM(i) "v_mfma_f32_32x32x16_bf16 v[80+16*" ST(i) ":95+16*" ST(i) "], v[4:7], v[150:153], 0\n"
#define FILLV "v_mov_b32 v4, 0x3f803f80\n v_mov_b32 v5, v4\n v_mov_b64 v[6:7], v[4:5]\n s_nop 1\n" FILLM(0) FILLM(1) FILLM(2) FILLM(3)
#define SPIN(lbl, off, tgt) \
  "s_mov_b32 s58, 0\n s_branch " lbl "1f\n" lbl ":\n s_sleep 1\n" lbl "1:\n ds_read_b32 v158, v159 offset:" ST(off) "\n s_add_u32 s58, s58, 0x400\n" \
  "s_cselect_b32 s59, 63, 0\n s_waitcnt lgkmcnt(0)\n v_readfirstlane_b32 s56, v158\n s_or_b32 s56, s56, s59\n" \
  "s_cmp_lt_u32 s56, " ST(tgt) "\n s_cbranch_scc1 " lbl "b\n"
#define SIG(off) "s_mov_b64 exec, 1\n ds_add_u32 v159, v163 offset:" ST(off) "\n s_mov_b64 exec, -1\n"
#define QGEMM \
  "v_mov_b32 v65, s52\n" \
  "v_mov_b32 v67, s52\n" \
  "v_mov_b32 v69, s52\n" \
  "v_mov_b32 v71, s52\n" \
  "v_mov_b32 v73, s52\n" \
  "v_mov_b32 v75, s52\n" \
  "v_mov_b32 v77, s52\n" \
  "v_mov_b32 v79, s52\n" \
  SPIN("21", 16, 4) \
  AST(3) \
  "ds_read_b128 v[20:23], v145 offset:0\n" \
  "ds_read_b64_tr_b16 v[16:17], v144 offset:0\n" \
  "ds_read_b64_tr_b16 v[18:19], v144 offset:512\n" \
  "ds_read_b128 v[28:31], v146 offset:0\n" \
  "ds_read_b64_tr_b16 v[24:25], v144 offset:2048\n" \
  "ds_read_b64_tr_b16 v[26:27], v144 offset:2560\n" \
  "ds_read_b128 v[36:39], v147 offset:0\n" \
  "ds_read_b64_tr_b16 v[32:33], v144 offset:4096\n" \
  "ds_read_b64_tr_b16 v[34:35], v144 offset:4608\n" \
  "ds_read_b128 v[44:47], v148 offset:0\n" \
  "ds_read_b64_tr_b16 v[40:41], v144 offset:6144\n" \
  "ds_read_b64_tr_b16 v[42:43], v144 offset:6656\n" \
  "s_waitcnt lgkmcnt(9)\n" \
  "v_mfma_f32_32x32x16_bf16 v[0:15], v[16:19], v[20:23], 0\n" \
  "ds_read_b128 v[20:23], v145 offset:8192\n" \
  "ds_read_b64_tr_b16 v[16:17], v144 offset:8192\n" \
  "ds_read_b64_tr_b16 v[18:19], v144 offset:8704\n" \
  "s_waitcnt lgkmcnt(9)\n" \
  "v_mfma_f32_32x32x16_bf16 v[0:15], v[24:27], v[28:31], v[0:15]\n" \
  "ds_read_b128 v[28:31], v146 offset:8192\n" \
  "ds_read_b64_tr_b16 v[24:25], v144 offset:10240\n" \
  "ds_read_b64_tr_b16 v[26:27], v144 offset:10752\n" \
  "s_waitcnt lgkmcnt(9)\n" \
  "v_mfma_f32_32x32x16_bf16 v[0:15], v[32:35], v[36:39], v[0:15]\n" \
  "ds_read_b128 v[36:39], v147 offset:8192\n" \
  "ds_read_b64_tr_b16 v[32:33], v144 offset:12288\n" \
  "ds_read_b64_tr_b16 v[34:35], v144 offset:12800\n" \
  "s_waitcnt lgkmcnt(9)\n" \
  "v_mfma_f32_32x32x16_bf16 v[0:15], v[40:43], v[44:47], v[0:15]\n" \
  "ds_read_b128 v[44:47], v148 offset:8192\n" \
  "ds_read_b64_tr_b16 v[40:41], v144 offset:14336\n" \
  "ds_read_b64_tr_b16 v[42:43], v144 offset:14848\n" \
  "s_waitcnt lgkmcnt(9)\n" \
  "v_mfma_f32_32x32x16_bf16 v[0:15], v[16:19], v[20:23], v[0:15]\n" \
  "ds_read_b128 v[20:23], v145 offset:16384\n" \
  "ds_read_b64_tr_b16 v[16:17], v144 offset:16384\n" \
  "ds_read_b64_tr_b16 v[18:19], v144 offset:16896\n" \
  "s_waitcnt lgkmcnt(9)\n" \
  "v_mfma_f32_32x32x16_bf16 v[0:15], v[24:27], v[28:31], v[0:15]\n" \
  "ds_read_b128 v[28:31], v146 offset:16384\n" \
  "ds_read_b64_tr_b16 v[24:25], v144 offset:18432\n" \
  "ds_read_b64_tr_b16 v[26:27], v144 offset:18944\n" \
  "s_waitcnt lgkmcnt(9)\n" \
  "v_mfma_f32_32x32x16_bf16 v[0:15], v[32:35], v[36:39], v[0:15]\n" \
  "ds_read_b128 v[36:39], v147 offset:16384\n" \
  "ds_read_b64_tr_b16 v[32:33], v144 offset:20480\n" \
  "ds_read_b64_tr_b16 v[34:35], v144 offset:20992\n" \
  "s_waitcnt lgkmcnt(9)\n" \
  "v_mfma_f32_32x32x16_bf16 v[0:15], v[40:43], v[44:47], v[0:15]\n" \
  "ds_read_b128 v[44:47], v148 offset:16384\n" \
  "ds_read_b64_tr_b16 v[40:41], v144 offset:22528\n" \
  "ds_read_b64_tr_b16 v[42:43], v144 offset:23040\n" \
  "s_waitcnt lgkmcnt(9)\n" \
  "v_mfma_f32_32x32x16_bf16 v[0:15], v[16:19], v[20:23], v[0:15]\n" \
  "ds_read_b128 v[20:23], v145 offset:24576\n" \
  "ds_read_b64_tr_b16 v[16:17], v144 offset:24576\n" \
  "ds_read_b64_tr_b16 v[18:19], v144 offset:25088\n" \
  "s_waitcnt lgkmcnt(9)\n" \
  "v_mfma_f32_32x32x16_bf16 v[0:15], v[24:27], v[28:31], v[0:15]\n" \
  "ds_read_b128 v[28:31], v146 offset:24576\n" \
  "ds_read_b64_tr_b16 v[24:25], v144 offset:26624\n" \
  "ds_read_b64_tr_b16 v[26:27], v144 offset:27136\n" \
  "s_waitcnt lgkmcnt(9)\n" \
  "v_mfma_f32_32x32x16_bf16 v[0:15], v[32:35], v[36:39], v[0:15]\n" \
  "ds_read_b128 v[36:39], v147 offset:24576\n" \
  "ds_read_b64_tr_b16 v[32:33], v144 offset:28672\n" \
  "ds_read_b64_tr_b16 v[34:35], v144 offset:29184\n" \
  "s_waitcnt lgkmcnt(9)\n" \
  "v_mfma_f32_32x32x16_bf16 v[0:15], v[40:43], v[44:47], v[0:15]\n" \
  "ds_read_b128 v[44:47], v148 offset:24576\n" \
  "ds_read_b64_tr_b16 v[40:41], v144 offset:30720\n" \
  "ds_read_b64_tr_b16 v[42:43], v144 offset:31232\n" \
  "s_waitcnt lgkmcnt(9)\n" \
  "v_mfma_f32_32x32x16_bf16 v[0:15], v[16:19], v[20:23], v[0:15]\n" \
  "s_waitcnt lgkmcnt(6)\n" \
  "v_mfma_f32_32x32x16_bf16 v[0:15], v[24:27], v[28:31], v[0:15]\n" \
  "s_waitcnt lgkmcnt(3)\n" \
  "v_mfma_f32_32x32x16_bf16 v[0:15], v[32:35], v[36:39], v[0:15]\n" \
  "s_waitcnt lgkmcnt(0)\n" \
  "v_mfma_f32_32x32x16_bf16 v[0:15], v[40:43], v[44:47], v[0:15]\n" \
  "s_waitcnt vmcnt(0)\n" \
  SIG(20) \
  SPIN("22", 20, 4) \
  "ds_read_b128 v[20:23], v145 offset:32768\n" \
  "ds_read_b64_tr_b16 v[16:17], v144 offset:32768\n" \
  "ds_read_b64_tr_b16 v[18:19], v144 offset:33280\n" \
  "ds_read_b128 v[28:31], v146 offset:32768\n" \
  "ds_read_b64_tr_b16 v[24:25], v144 offset:34816\n" \
  "ds_read_b64_tr_b16 v[26:27], v144 offset:35328\n" \
  "ds_read_b128 v[36:39], v147 offset:32768\n" \
  "ds_read_b64_tr_b16 v[32:33], v144 offset:36864\n" \
  "ds_read_b64_tr_b16 v[34:35], v144 offset:37376\n" \
  "ds_read_b128 v[44:47], v148 offset:32768\n" \
  "ds_read_b64_tr_b16 v[40:41], v144 offset:38912\n" \
  "ds_read_b64_tr_b16 v[42:43], v144 offset:39424\n" \
  "s_waitcnt lgkmcnt(9)\n" \
  "v_mfma_f32_32x32x16_bf16 v[0:15], v[16:19], v[20:23], v[0:15]\n" \
  "ds_read_b128 v[20:23], v145 offset:40960\n" \
  "ds_read_b64_tr_b16 v[16:17], v144 offset:40960\n" \
  "ds_read_b64_tr_b16 v[18:19], v144 offset:41472\n" \
  "s_waitcnt lgkmcnt(9)\n" \
  "v_mfma_f32_32x32x16_bf16 v[0:15], v[24:27], v[28:31], v[0:15]\n" \
  "ds_read_b128 v[28:31], v146 offset:40960\n" \
  "ds_read_b64_tr_b16 v[24:25], v144 offset:43008\n" \
  "ds_read_b64_tr_b16 v[26:27], v144 offset:43520\n" \
  "s_waitcnt lgkmcnt(9)\n" \
  "v_mfma_f32_32x32x16_bf16 v[0:15], v[32:35], v[36:39], v[0:15]\n" \
  "ds_read_b128 v[36:39], v147 offset:40960\n" \
  "ds_read_b64_tr_b16 v[32:33], v144 offset:45056\n" \
  "ds_read_b64_tr_b16 v[34:35], v144 offset:45568\n" \
  "s_waitcnt lgkmcnt(9)\n" \
  "v_mfma_f32_32x32x16_bf16 v[0:15], v[40:43], v[44:47], v[0:15]\n" \
  "ds_read_b128 v[44:47], v148 offset:40960\n" \
  "ds_read_b64_tr_b16 v[40:41], v144 offset:47104\n" \
  "ds_read_b64_tr_b16 v[42:43], v144 offset:47616\n" \
  "s_waitcnt lgkmcnt(9)\n" \
  "v_mfma_f32_32x32x16_bf16 v[0:15], v[16:19], v[20:23], v[0:15]\n" \
  "ds_read_b128 v[20:23], v145 offset:49152\n" \
  "ds_read_b64_tr_b16 v[16:17], v144 offset:49152\n" \
  "ds_read_b64_tr_b16 v[18:19], v144 offset:49664\n" \
  "s_waitcnt lgkmcnt(9)\n" \
  "v_mfma_f32_32x32x16_bf16 v[0:15], v[24:27], v[28:31], v[0:15]\n" \
  "ds_read_b128 v[28:31], v146 offset:49152\n" \
  "ds_read_b64_tr_b16 v[24:25], v144 offset:51200\n" \
  "ds_read_b64_tr_b16 v[26:27], v144 offset:51712\n" \
  "s_waitcnt lgkmcnt(9)\n" \
  "v_mfma_f32_32x32x16_bf16 v[0:15], v[32:35], v[36:39], v[0:15]\n" \
  "ds_read_b128 v[36:39], v147 offset:49152\n" \
  "ds_read_b64_tr_b16 v[32:33], v144 offset:53248\n" \
  "ds_read_b64_tr_b16 v[34:35], v144 offset:53760\n" \
  POLL \
  "s_waitcnt lgkmcnt(9)\n" \
  "v_mfma_f32_32x32x16_bf16 v[0:15], v[40:43], v[44:47], v[0:15]\n" \
  "ds_read_b128 v[44:47], v148 offset:49152\n" \
  "ds_read_b64_tr_b16 v[40:41], v144 offset:55296\n" \
  "ds_read_b64_tr_b16 v[42:43], v144 offset:55808\n" \
  "s_waitcnt lgkmcnt(9)\n" \
  "v_mfma_f32_32x32x16_bf16 v[0:15], v[16:19], v[20:23], v[0:15]\n" \
  "ds_read_b128 v[20:23], v145 offset:57344\n" \
  "ds_read_b64_tr_b16 v[16:17], v144 offset:57344\n" \
  "ds_read_b64_tr_b16 v[18:19], v144 offset:57856\n" \
  "s_waitcnt lgkmcnt(9)\n" \
  "v_mfma_f32_32x32x16_bf16 v[0:15], v[24:27], v[28:31], v[0:15]\n" \
  "ds_read_b128 v[28:31], v146 offset:57344\n" \
  "ds_read_b64_tr_b16 v[24:25], v144 offset:59392\n" \
  "ds_read_b64_tr_b16 v[26:27], v144 offset:59904\n" \
  "s_waitcnt lgkmcnt(9)\n" \
  "v_mfma_f32_32x32x16_bf16 v[0:15], v[32:35], v[36:39], v[0:15]\n" \
  "ds_read_b128 v[36:39], v147 offset:57344\n" \
  "ds_read_b64_tr_b16 v[32:33], v144 offset:61440\n" \
  "ds_read_b64_tr_b16 v[34:35], v144 offset:61952\n" \
  "s_waitcnt lgkmcnt(9)\n" \
  "v_mfma_f32_32x32x16_bf16 v[0:15], v[40:43], v[44:47], v[0:15]\n" \
  "ds_read_b128 v[44:47], v148 offset:57344\n" \
  "ds_read_b64_tr_b16 v[40:41], v144 offset:63488\n" \
  "ds_read_b64_tr_b16 v[42:43], v144 offset:64000\n" \
  "s_waitcnt lgkmcnt(9)\n" \
  "v_mfma_f32_32x32x16_bf16 v[0:15], v[16:19], v[20:23], v[0:15]\n" \
  "s_waitcnt lgkmcnt(6)\n" \
  "v_mfma_f32_32x32x16_bf16 v[0:15], v[24:27], v[28:31], v[0:15]\n" \
  "s_waitcnt lgkmcnt(3)\n" \
  "v_mfma_f32_32x32x16_bf16 v[0:15], v[32:35], v[36:39], v[0:15]\n" \
  "s_waitcnt lgkmcnt(0)\n" \
  "v_mfma_f32_32x32x16_bf16 v[0:15], v[40:43], v[44:47], v[0:15]\n" \
  "s_nop 11\n" \
  "v_cvt_pk_bf16_f32 v64, v0, v1\n" \
  "v_cvt_pk_bf16_f32 v66, v2, v3\n" \
  "v_cvt_pk_bf16_f32 v68, v4, v5\n" \
  "v_cvt_pk_bf16_f32 v70, v6, v7\n" \
  "v_cvt_pk_bf16_f32 v72, v8, v9\n" \
  "v_cvt_pk_bf16_f32 v74, v10, v11\n" \
  "v_cvt_pk_bf16_f32 v76, v12, v13\n" \
  "v_cvt_pk_bf16_f32 v78, v14, v15\n" \
  "ds_write2_b32 v164, v64, v66 offset0:0 offset1:1\n" \
  "ds_write2_b32 v164, v68, v70 offset0:128 offset1:129\n" \
  "ds_write2_b32 v164, v72, v74 offset0:32 offset1:33\n" \
  "ds_write2_b32 v164, v76, v78 offset0:160 offset1:161\n" \
  SIG(36) \
  SIG(32) \
  AST(4) \
  "s_waitcnt vmcnt(0)\n" \
  "v_cmp_ne_u64 vcc, s[52:53], v[150:151]\n" \
  "buffer_store_dwordx4 v[64:67], v149, %[rs], 0 offen offset:0 sc1\n" \
  "buffer_store_dwordx4 v[68:71], v149, %[rs], 0 offen offset:256 sc1\n" \
  "buffer_store_dwordx4 v[72:75], v149, %[rs], 0 offen offset:512 sc1\n" \
  "buffer_store_dwordx4 v[76:79], v149, %[rs], 0 offen offset:768 sc1\n" \
  "s_cbranch_vccnz 4f\n" \
  LOADKV \
  "s_branch 7f\n"
#define DMX "s_add_u32 %[blk], %[blk], 1\n s_and_b32 %[blk], %[blk], 3\n s_lshl_b32 %[t0], %[blk], 11\n s_add_u32 m0, %[xim], %[t0]\n" \
  "s_lshl_b32 %[t0], %[blk], 14\n s_add_u32 %[t0], %[t0], %[sx]\n buffer_load_dwordx4 %[xs0], %[rx], %[t0] offen lds\n" \
  "s_add_u32 m0, m0, 1024\n s_nop 0\n buffer_load_dwordx4 %[xs1], %[rx], %[t0] offen lds\n"
#define DMA "buffer_load_dwordx4 %[we], %[rw], %[sw0] offen lds\n s_add_u32 m0, m0, 1024\n s_add_u32 %[sw0], %[sw0], 8192\n"
#define GATEW "s_mov_b32 %[t1], 0\n 13:\n ds_read_b32 v64, v159 offset:8\n s_add_u32 %[t1], %[t1], 0x1000000\n s_cbranch_scc1 14f\n s_waitcnt lgkmcnt(0)\n" \
        "v_readfirstlane_b32 %[t0], v64\n s_cmp_lt_u32 %[t0], " ST(GATEN) "\n s_cbranch_scc1 13b\n 14:\n s_waitcnt lgkmcnt(0)\n"
#define AST(n)
#define ASTCL
#define RST0
#define RST
#define RSTOP
#define ASTOUT(n) do {} while (0)
#define CL10(a) "v" ST(a) "0", "v" ST(a) "1", "v" ST(a) "2", "v" ST(a) "3", "v" ST(a) "4", "v" ST(a) "5", "v" ST(a) "6", "v" ST(a) "7", "v" ST(a) "8", "v" ST(a) "9"
static_assert(PBUF == 16384 && LBUF == 2048 && L_L % 8192 == 0 && L_O % 4096 == 0 && (L_CTL + C_P) % 16 == 0 && C_P - C_SVC == 96 && WS_VF - WS_KF == 65536 && L_EX - L_QR == 8192 && C_KV == 8 && C_WQ == 16 && C_Q == 32, "offsets are spelled out in the asm");
typedef u32 u32x16 __attribute__((ext_vector_type(16)));
typedef u32 u32x32 __attribute__((ext_vector_type(32)));

DI void attn_role(lptr lds, int m, int w, int lane) {
  const int i = m >> 3, j = m & 7, T0 = 64 * i;
  STAMP_INIT(w);
  STAMP(0);
  CSTAMP(30);
  u32 xs0 = (lane >> 3) * 1024 + (((lane & 7) ^ (lane >> 4)) << 4), xs1 = (xs0 ^ 64) + 8192, lo16 = lane * 16;
  u32 sk = (T0 + 16 * (j >> 1)) * 1024 + 128 * w;
  asm volatile("" : "+v"(xs0), "+v"(xs1), "+v"(lo16), "+s"(sk));
  const Params p = wait_params();
  const rsrc_t rs = ws_rsrc(p.ws);
  const u32 kp = WS_KF + w * 8192, vp = WS_VF + w * 8192;
  {
    const u32 l16 = lane & 15, g = lane >> 4, r32 = lane & 31, h = lane >> 5, q4 = l16 >> 2, pp = l16 & 3;
    const u32 lb = (u32)(uintptr_t)lds, ex = lb + L_EX + w * 2048, ctl = lb + L_CTL;
    const rsrc_t rx = __builtin_amdgcn_make_buffer_rsrc((void*)p.x, 0, SEQ * DM * 2, 0x00020000);
    const rsrc_t rw = __builtin_amdgcn_make_buffer_rsrc((void*)p.wq, 0, DM * DM * 2, 0x00020000);
    const rsrc_t rk = __builtin_amdgcn_make_buffer_rsrc((void*)((j & 1) ? p.wv : p.wk), 0, DM * 32, 0x00020000);
    const u32 xim = lb + L_XY + w * 8192;
    u32 keep;
    u64 epoch;
    {
      asm volatile(
        "s_mov_b32 %[keep], m0\n s_mov_b32 m0, %[ex]\n s_setprio 3\n s_nop 2\n"
        "buffer_load_dwordx4 %[lo16], %[rk], %[so] offen lds\n buffer_load_dwordx4 %[lo16], %[rk], %[so] offen offset:1024 lds\n"
        "s_mov_b32 m0, %[xim]\n s_nop 0\n buffer_load_dwordx4 %[xs0], %[rx], %[sk] offen lds\n"
        "s_add_u32 m0, m0, 1024\n s_nop 0\n buffer_load_dwordx4 %[xs1], %[rx], %[sk] offen lds\n"
        : [keep] "=&s"(keep)
        : [lo16] "v"(lo16), [xs0] "v"(xs0), [xs1] "v"(xs1), [rk] "s"(rk), [rx] "s"(rx), [so] "s"(w * 2048), [ex] "s"(ex), [xim] "s"(xim + (j >> 1) * 2048),
          [sk] "s"(sk)
        : "memory", "scc");
      __builtin_amdgcn_sched_barrier(0);
    }
    const u32 xr0 = xim + (j >> 1) * 2048 + l16 * 128 + ((g ^ (l16 >> 1)) << 4);
    const u32 th = (w >> 1) & 1, nh = w & 1, jc = 4 * th + (r32 >> 3);
    u32x8 ev;
    ev[0] = lb + L_WQ + (8 * h + q4) * 128 + 8 * (pp & 1) + (((4 * nh + 2 * (g & 1) + (pp >> 1)) ^ ((q4 & 2) << 1)) << 4);
    ev[1] = lb + L_XY + (32 * th + r32) * 128 + ((h ^ ((r32 >> 1) & 7)) << 4);
    ev[2] = ev[1] ^ 32; ev[3] = ev[1] ^ 64; ev[4] = ev[1] ^ 96;
    const u32 slot = (j - jc - 1) & 7;
    ev[5] = slot < 2 ? WS_QF + ((8 * i + jc) * 2 + slot) * 2048 + nh * 1024 + h * 128 + (r32 & 7) * 16 : 0x7f000000u;
    ev[6] = (lane == 16) ? 0x3afe3f80u : 0u; ev[7] = 0u;
    u32x16 fv;
    fv[0] = 0u; fv[1] = 0u; fv[2] = 0u; fv[3] = 0u;
    fv[4] = 0xddddu;
    fv[5] = lb + L_QR + lo16;
    fv[6] = 0u;
    fv[7] = ctl;
    fv[8] = lo16;
    fv[9] = (h * 16 + (r32 & 15)) * 16;
    fv[10] = ctl + C_P;
    fv[11] = 1u;
    fv[12] = lb + L_EX + jc * 1024 + nh * 256 + (r32 & 7) * 16 + 8 * h;
    fv[13] = lb + L_PART + w * PW + lo16;
    fv[14] = lb + L_L + w * 4 + r32 * 64 + h * 32;
    fv[15] = (r32 >> 3) * 512 + h * 256 + (r32 & 7) * 16;
    u32x8 sv;
    sv[0] = 0xffffu; sv[1] = 0xffffu;
    sv[2] = kp + 4096; sv[3] = vp; sv[4] = vp + 4096;
    sv[5] = kp;
    sv[6] = WS_GO + (m >> 5) * 1024 + w * 128;
    sv[7] = WS_QF + m * 4096;
    {
      const u32 we = (lane >> 3) * 1024 + (((lane & 7) ^ ((g & 1) << 2)) << 4);
      u32 sw0 = 64 * w * 1024 + 128 * j, blk = j >> 1, t0, t1;
      asm volatile(
        VM(2)
        "ds_read_b64_tr_b16 v[64:65], %[tr]\n ds_read_b64_tr_b16 v[66:67], %[tr] offset:128\n"
        "ds_read_b64_tr_b16 v[68:69], %[tr] offset:1024\n ds_read_b64_tr_b16 v[70:71], %[tr] offset:1152\n"
        VM(0)
        "ds_read_b128 v[72:75], %[r0]\n ds_read_b128 v[76:79], %[r1]\n"
        "s_cmp_lg_u32 %[isv], 0\n s_mov_b32 m0, %[sm0]\n s_waitcnt lgkmcnt(0)\n s_cbranch_scc1 11f\n"
        "v_mfma_f32_16x16x32_bf16 v[60:63], v[64:67], v[72:75], 0\n v_mfma_f32_16x16x32_bf16 v[60:63], v[68:71], v[76:79], v[60:63]\n s_branch 12f\n"
        "11:\n v_mfma_f32_16x16x32_bf16 v[60:63], v[72:75], v[64:67], 0\n v_mfma_f32_16x16x32_bf16 v[60:63], v[76:79], v[68:71], v[60:63]\n"
        "12:\n s_nop 7\n ds_write_b128 %[kvp], v[60:63]\n"
        SIG(8) "s_setprio 1\n"
        GATEW "buffer_load_dwordx2 v[46:47], off, %[rs], 0 sc1\n" DMA DMA DMA DMA DMA DMA DMA DMA DMX DMX DMX
        "s_mov_b32 m0, %[keep]\n"
        : [sw0] "+&s"(sw0), [blk] "+&s"(blk), "=&{v[46:47]}"(epoch), [t0] "=&s"(t0), [t1] "=&s"(t1),
          "+&{v[144:151]}"(ev), "+&{v[152:167]}"(fv), "+&{s[36:43]}"(sv)
        : [tr] "v"(ex + (8 * g + q4) * 32 + 8 * pp), [r0] "v"(xr0), [r1] "v"(xr0 ^ 64), [xs0] "v"(xs0), [xs1] "v"(xs1),
          [kvp] "v"(lb + L_KVP + w * 1024 + lo16), [we] "v"(we), [isv] "s"(j & 1), [rw] "s"(rw), [rx] "s"(rx), [rs] "s"(rs), [xim] "s"(xim),
          [sx] "s"(T0 * 1024 + 128 * w), [sm0] "s"(lb + L_WQ + w * 8192), [keep] "s"(keep)
        : "memory", "scc", CL10(6), CL10(7));
    }
    asm volatile(
      AST(1)
      FILLV
      VM(14) "v_readfirstlane_b32 s52, v46\n v_readfirstlane_b32 s53, v47\n"
      "s_mul_i32 s52, s52, " ST(TAG_A0) "\n s_add_u32 s52, s52, " ST(TAG_C0) "\n s_mul_i32 s53, s53, " ST(TAG_A1) "\n s_add_u32 s53, s53, " ST(TAG_C1) "\n"
      "s_cmp_lt_u32 %[wv], 4\n s_cbranch_scc0 1f\n"
      VM(0) SIG(16) AST(2) "s_branch 4f\n"
      "1:\n"
      QGEMM
      "4:\n s_mov_b32 s57, 0x200000\n"
      "5:\n" POLL
      "s_sub_u32 s57, s57, 1\n s_cbranch_scc1 6f\n"
      "s_waitcnt vmcnt(0)\n v_cmp_ne_u64 vcc, s[52:53], v[150:151]\n s_cbranch_vccz 6f\n s_sleep 3\n s_branch 5b\n"
      "6:\n s_waitcnt vmcnt(0)\n" AST(5)
      LOADKV
      "7:\n" AST(6)
      SPIN("27", 36, 4)
      "v_add_u32 v164, %[qo], v160\n ds_read_b128 v[144:147], v164\n"
      "s_waitcnt vmcnt(8) lgkmcnt(0)\n"
      "s_setprio 3\n" AST(7) QK(16, 0, 144) ZO(144)
      "s_nop 9\n"
      ROUND0 AST(9)
      "s_mov_b32 s60, 3\n s_mov_b32 s61, 0\n"
      RST0 "8:\n" RST
      PRIO("s_cbranch_scc1", "4") ROUND(148, 144, 0, 0, 0, IF1("80") LDQ(144, 2048) "80:\n",
        "s_cmp_eq_u32 s60, 3\n s_cbranch_scc1 81f\n" CHK("5") RDQ(144, 2048) "81:\n", LG0 IF1("82") QCHK(144, 2048, "83") "82:\n")
      "s_sub_u32 s60, s60, 1\n s_cbranch_scc1 9f\n"
      RST PRIO("s_cbranch_scc0", "6") ROUND(144, 148, 16384, 2048, 4, "", CHK("7") RDQ(148, 3072), LG0)
      "v_add_u32 v157, 0x800, v157\n v_xor_b32 v165, 0x8000, v165\n v_xor_b32 v166, 0x1000, v166\n v_xor_b32 v162, 8, v162\n"
      "s_branch 8b\n"
      "9:\n" RST
      PV(15, 40)
      "s_nop 11\n"
      PUT(16384, 2048, 4)
      : "+&{v[144:151]}"(ev), "+&{v[152:167]}"(fv), "+&{s[36:43]}"(sv), "+&{v[46:47]}"(epoch)
      : RSTOP [rs] "s"(rs), [wv] "s"(w), [qo] "s"(lb + L_EX + j * 1024)
      : "memory", "scc", "vcc", CL10(), CL10(1), CL10(2), CL10(3), "v40", "v41", "v42", "v43", "v44", "v45", "v48", "v49", CL10(5), CL10(6),
        CL10(7), CL10(8), CL10(9), CL10(10), CL10(11), CL10(12), CL10(13), "v140", "v141", "v142", "v143",
        "s52", "s53", "s56", "s57", "s58", "s59", "s60", "s61" ASTCL);
    ASTOUT(1); ASTOUT(2); ASTOUT(3); ASTOUT(4); ASTOUT(5); ASTOUT(6); ASTOUT(7); ASTOUT(8); ASTOUT(9);
  }
  STAMP(3);
  CSTAMP(31);
  STAMP_DUMP();
  blk_barrier();
  if (__builtin_amdgcn_readfirstlane(lds_ld<u32>(lds + L_CTL + C_BAD)) != 0u) {
    const f32x16 zero16 = {0.f, 0.f, 0.f, 0.f, 0.f, 0.f, 0.f, 0.f, 0.f, 0.f, 0.f, 0.f, 0.f, 0.f, 0.f, 0.f};
    const u32 ln = lane_id();
    int idx;
    asm volatile("v_mov_b32 %0, 0xdddd" : "=v"(idx));
    const u32 rr = ln & 31, hh = ln >> 5;
    const u32 lo16 = ln * 16, vo16 = (hh * 16 + (rr & 15)) * 16;
    const u32 fill = (rr == 16) ? 0x3f803f80u : 0u;
    const u32x4 fill4 = {fill, fill, fill, fill};
#pragma unroll 1
    for (int r = 0; r < 8; ++r) {
      const bf16x8 qf = bld8(rs, lo16, WS_QP + (m * 8 + r) * 1024);
      float mx = -INFINITY;
#pragma unroll 1
      for (int t = 0; t < 8; ++t) {
        f32x16 s = mfma32(bld8(rs, lo16, kp + t * 1024), qf, zero16);
#pragma unroll
        for (int e = 0; e < 16; ++e) mx = fmaxf(mx, s[e]);
      }
      { auto sw2 = __builtin_amdgcn_permlane32_swap(__float_as_uint(mx), __float_as_uint(mx), false, false);
        mx = fmaxf(__uint_as_float(sw2[0]), __uint_as_float(sw2[1])); }
      if (hh == 0) lds_st<float>(lds + L_MX + rr * 32 + w * 4, mx);
      blk_barrier();
      f32x4 m0 = lds_ld<f32x4>(lds + L_MX + rr * 32), m1 = lds_ld<f32x4>(lds + L_MX + rr * 32 + 16);
      mx = fmaxf(fmaxf(fmaxf(m0[0], m0[1]), fmaxf(m0[2], m0[3])), fmaxf(fmaxf(m1[0], m1[1]), fmaxf(m1[2], m1[3])));
      f32x16 o = zero16;
#pragma unroll 1
      for (int t = 0; t < 8; ++t) {
        f32x16 s = mfma32(bld8(rs, lo16, kp + t * 1024), qf, zero16);
        u32x4 v0 = bld4(rs, vo16, vp + t * 1024), v1 = bld4(rs, vo16, vp + t * 1024 + 512);
        if (rr >= 16) { v0 = fill4; v1 = fill4; }
#pragma unroll
        for (int e = 0; e < 16; ++e) s[e] = __builtin_amdgcn_exp2f(s[e] - mx);
        o = pv_tile(o, __builtin_bit_cast(bf16x8, v0), __builtin_bit_cast(bf16x8, v1), s, idx);
      }
      lptr pq = lds + L_PART + (r & 1) * PBUF + w * PW + lo16;
      f32x4 a = {o[0], o[1], o[2], o[3]}, b = {o[4], o[5], o[6], o[7]};
      lds_st<f32x4>(pq, a);
      lds_st<f32x4>(pq + 1024, b);
      if (hh == 0) lds_st<float>(lds + L_L + (r & 1) * LBUF + rr * 64 + w * 4, o[8]);
      blk_barrier();
    }
  }
}

DI f32x2 cmul(f32x2 a, f32x2 b) {
  f32x2 r;
  r[0] = __builtin_fmaf(a[0], b[0], -(a[1] * b[1]));
  r[1] = __builtin_fmaf(a[0], b[1], a[1] * b[0]);
  return r;
}
DI f32x2 cexp_t(float lr, float fr, float t) {
  const float mag = __builtin_amdgcn_exp2f(lr * t);
  const float ph = __builtin_amdgcn_fractf(fr * t);
  f32x2 r = {mag * __builtin_amdgcn_cosf(ph), mag * __builtin_amdgcn_sinf(ph)};
  return r;
}
template <int N> DI float ror16(float v) { return __uint_as_float(__builtin_amdgcn_update_dpp(0u, __float_as_uint(v), 0x120 + N, 0xf, 0xf, false)); }
DI float row_sum(float v) { v += ror16<8>(v); v += ror16<4>(v); v += ror16<2>(v); v += ror16<1>(v); return v; }
DI float wave_max(float v) {
  v = fmaxf(v, ror16<8>(v)); v = fmaxf(v, ror16<4>(v)); v = fmaxf(v, ror16<2>(v)); v = fmaxf(v, ror16<1>(v));
  const u32 u = __float_as_uint(v);
  const u32 a = __builtin_amdgcn_readlane(u, 0), b = __builtin_amdgcn_readlane(u, 16), c = __builtin_amdgcn_readlane(u, 32), d = __builtin_amdgcn_readlane(u, 48);
  return __uint_as_float(max(max(a, b), max(c, d)));
}

#define SST
#define SVC_MAIN \
  "s_mov_b32 s55, 0\n" \
  "s_branch 102f\n" \
  "101:\n" \
  "s_sleep 2\n" \
  "s_add_u32 s55, s55, 0x400\n" \
  "s_cbranch_scc1 103f\n" \
  "102:\n" \
  "ds_read_b32 v112, v156 offset:32\n" \
  "s_waitcnt lgkmcnt(0)\n" \
  "v_readfirstlane_b32 s54, v112\n" \
  "s_cmp_lt_u32 s54, 6\n" \
  "s_cbranch_scc1 101b\n" \
  "103:\n" \
  SST \
  "ds_read_b128 v[80:83], v142 offset:8192\n" \
  "ds_read_b128 v[84:87], v142 offset:9216\n" \
  "ds_write_b32 v136, v0 offset:0\n" \
  "ds_write_b32 v136, v1 offset:528\n" \
  "ds_write_b32 v136, v2 offset:1056\n" \
  "ds_write_b32 v136, v3 offset:1584\n" \
  "ds_write_b32 v136, v4 offset:2112\n" \
  "ds_write_b32 v136, v5 offset:2640\n" \
  "ds_write_b32 v136, v6 offset:3168\n" \
  "ds_write_b32 v136, v7 offset:3696\n" \
  "ds_write_b32 v136, v8 offset:4224\n" \
  "ds_write_b32 v136, v9 offset:4752\n" \
  "ds_write_b32 v136, v10 offset:5280\n" \
  "ds_write_b32 v136, v11 offset:5808\n" \
  "ds_write_b32 v136, v12 offset:6336\n" \
  "ds_write_b32 v136, v13 offset:6864\n" \
  "ds_write_b32 v136, v14 offset:7392\n" \
  "ds_write_b32 v136, v15 offset:7920\n" \
  "ds_write_b32 v136, v16 offset:8448\n" \
  "ds_write_b32 v136, v17 offset:8976\n" \
  "ds_write_b32 v136, v18 offset:9504\n" \
  "ds_write_b32 v136, v19 offset:10032\n" \
  "ds_write_b32 v136, v20 offset:10560\n" \
  "ds_write_b32 v136, v21 offset:11088\n" \
  "ds_write_b32 v136, v22 offset:11616\n" \
  "ds_write_b32 v136, v23 offset:12144\n" \
  "ds_write_b32 v136, v24 offset:12672\n" \
  "ds_write_b32 v136, v25 offset:13200\n" \
  "ds_write_b32 v136, v26 offset:13728\n" \
  "ds_write_b32 v136, v27 offset:14256\n" \
  "ds_write_b32 v136, v28 offset:14784\n" \
  "ds_write_b32 v136, v29 offset:15312\n" \
  "ds_write_b32 v136, v30 offset:15840\n" \
  "s_waitcnt lgkmcnt(14)\n" \
  "buffer_store_dwordx4 v[80:83], v160, %[rs], %[qs0] offen sc1\n" \
  "buffer_store_dwordx4 v[84:87], v160, %[rs], %[qs1] offen sc1\n" \
  "s_mov_b32 s54, 0\n" \
  "buffer_store_dwordx4 v[64:67], v143, %[rs], s54 offen sc1\n" \
  "s_add_u32 s54, s54, 0x8000\n" \
  "buffer_store_dwordx4 v[68:71], v143, %[rs], s54 offen sc1\n" \
  "s_add_u32 s54, s54, 0x8000\n" \
  "buffer_store_dwordx4 v[72:75], v143, %[rs], s54 offen sc1\n" \
  "s_add_u32 s54, s54, 0x8000\n" \
  "buffer_store_dwordx4 v[76:79], v143, %[rs], s54 offen sc1\n" \
  "s_add_u32 s54, s54, 0x8000\n" \
  "ds_write_b32 v136, v31 offset:16368\n" \
  "ds_write_b32 v136, v32 offset:16896\n" \
  "ds_write_b32 v136, v33 offset:17424\n" \
  "ds_write_b32 v136, v34 offset:17952\n" \
  "ds_write_b32 v136, v35 offset:18480\n" \
  "ds_write_b32 v136, v36 offset:19008\n" \
  "ds_write_b32 v136, v37 offset:19536\n" \
  "ds_write_b32 v136, v38 offset:20064\n" \
  "ds_write_b32 v136, v39 offset:20592\n" \
  "ds_write_b32 v136, v40 offset:21120\n" \
  "ds_write_b32 v136, v41 offset:21648\n" \
  "ds_write_b32 v136, v42 offset:22176\n" \
  "ds_write_b32 v136, v43 offset:22704\n" \
  "ds_write_b32 v136, v44 offset:23232\n" \
  "ds_write_b32 v136, v45 offset:23760\n" \
  "ds_write_b32 v136, v46 offset:24288\n" \
  "ds_write_b32 v136, v47 offset:24816\n" \
  "ds_write_b32 v136, v48 offset:25344\n" \
  "ds_write_b32 v136, v49 offset:25872\n" \
  "ds_write_b32 v136, v50 offset:26400\n" \
  "ds_write_b32 v136, v51 offset:26928\n" \
  "ds_write_b32 v136, v52 offset:27456\n" \
  "ds_write_b32 v136, v53 offset:27984\n" \
  "ds_write_b32 v136, v54 offset:28512\n" \
  "ds_write_b32 v136, v55 offset:29040\n" \
  "ds_write_b32 v136, v56 offset:29568\n" \
  "ds_write_b32 v136, v57 offset:30096\n" \
  "ds_write_b32 v136, v58 offset:30624\n" \
  "ds_write_b32 v136, v59 offset:31152\n" \
  "ds_write_b32 v136, v60 offset:31680\n" \
  "ds_write_b32 v136, v61 offset:32208\n" \
  "ds_write_b32 v136, v62 offset:32736\n" \
  "s_mov_b64 exec, 1\n" \
  "ds_add_u32 v163, v157 offset:48\n" \
  "s_mov_b64 exec, -1\n" \
  "s_mov_b32 s55, 0\n" \
  "s_branch 105f\n" \
  "104:\n" \
  "s_sleep 1\n" \
  "s_add_u32 s55, s55, 0x400\n" \
  "s_cbranch_scc1 106f\n" \
  "105:\n" \
  "ds_read_b32 v112, v156 offset:8\n" \
  "s_waitcnt lgkmcnt(0)\n" \
  "v_readfirstlane_b32 s54, v112\n" \
  "s_cmp_lt_u32 s54, 10\n" \
  "s_cbranch_scc1 104b\n" \
  "106:\n" \
  "s_mov_b32 s55, 0\n" \
  "s_branch 108f\n" \
  "107:\n" \
  "s_sleep 1\n" \
  "s_add_u32 s55, s55, 0x400\n" \
  "s_cbranch_scc1 109f\n" \
  "108:\n" \
  "ds_read_b32 v112, v163 offset:48\n" \
  "s_waitcnt lgkmcnt(0)\n" \
  "v_readfirstlane_b32 s54, v112\n" \
  "s_cmp_lt_u32 s54, 2\n" \
  "s_cbranch_scc1 107b\n" \
  "109:\n" \
  SST \
  "ds_read_b128 v[0:3], v137 offset:0\n" \
  "ds_read_b128 v[80:83], v138 offset:0\n" \
  "ds_read_b128 v[84:87], v139 offset:0\n" \
  "ds_read_b128 v[88:91], v140 offset:0\n" \
  "ds_read_b128 v[92:95], v141 offset:0\n" \
  "ds_read_b128 v[4:7], v137 offset:64\n" \
  "ds_read_b128 v[96:99], v138 offset:64\n" \
  "ds_read_b128 v[100:103], v139 offset:64\n" \
  "ds_read_b128 v[104:107], v140 offset:64\n" \
  "ds_read_b128 v[108:111], v141 offset:64\n" \
  "ds_read_b128 v[8:11], v137 offset:128\n" \
  "ds_read_b128 v[112:115], v138 offset:128\n" \
  "ds_read_b128 v[116:119], v139 offset:128\n" \
  "ds_read_b128 v[120:123], v140 offset:128\n" \
  "ds_read_b128 v[124:127], v141 offset:128\n" \
  "s_waitcnt lgkmcnt(10)\n" \
  "v_mfma_f32_16x16x32_f16 v[64:67], v[80:83], v[0:3], 0\n" \
  "v_mfma_f32_16x16x32_f16 v[68:71], v[84:87], v[0:3], 0\n" \
  "v_mfma_f32_16x16x32_f16 v[72:75], v[88:91], v[0:3], 0\n" \
  "v_mfma_f32_16x16x32_f16 v[76:79], v[92:95], v[0:3], 0\n" \
  "ds_read_b128 v[12:15], v137 offset:192\n" \
  "ds_read_b128 v[80:83], v138 offset:192\n" \
  "ds_read_b128 v[84:87], v139 offset:192\n" \
  "ds_read_b128 v[88:91], v140 offset:192\n" \
  "ds_read_b128 v[92:95], v141 offset:192\n" \
  "s_waitcnt vmcnt(0)\n" \
  "s_mov_b64 exec, 1\n" \
  "ds_add_rtn_u32 v63, v156, v157 offset:12\n" \
  "s_waitcnt lgkmcnt(0)\n" \
  "v_readfirstlane_b32 s54, v63\n" \
  "v_mov_b32 v63, 1\n" \
  "s_cmp_eq_u32 s54, 3\n" \
  "s_cselect_b32 s59, 1, 0\n" \
  "s_mov_b32 exec_lo, s59\n" \
  "buffer_atomic_add v63, off, %[rs], s43 offset:256 sc0\n" \
  "s_mov_b64 exec, -1\n" \
  "v_mfma_f32_16x16x32_f16 v[64:67], v[96:99], v[4:7], v[64:67]\n" \
  "v_mfma_f32_16x16x32_f16 v[68:71], v[100:103], v[4:7], v[68:71]\n" \
  "v_mfma_f32_16x16x32_f16 v[72:75], v[104:107], v[4:7], v[72:75]\n" \
  "v_mfma_f32_16x16x32_f16 v[76:79], v[108:111], v[4:7], v[76:79]\n" \
  "ds_read_b128 v[16:19], v137 offset:256\n" \
  "ds_read_b128 v[96:99], v138 offset:256\n" \
  "ds_read_b128 v[100:103], v139 offset:256\n" \
  "ds_read_b128 v[104:107], v140 offset:256\n" \
  "ds_read_b128 v[108:111], v141 offset:256\n" \
  "v_mfma_f32_16x16x32_f16 v[64:67], v[112:115], v[8:11], v[64:67]\n" \
  "v_mfma_f32_16x16x32_f16 v[68:71], v[116:119], v[8:11], v[68:71]\n" \
  "v_mfma_f32_16x16x32_f16 v[72:75], v[120:123], v[8:11], v[72:75]\n" \
  "v_mfma_f32_16x16x32_f16 v[76:79], v[124:127], v[8:11], v[76:79]\n" \
  "ds_read_b128 v[20:23], v137 offset:320\n" \
  "ds_read_b128 v[112:115], v138 offset:320\n" \
  "ds_read_b128 v[116:119], v139 offset:320\n" \
  "ds_read_b128 v[120:123], v140 offset:320\n" \
  "ds_read_b128 v[124:127], v141 offset:320\n" \
  "v_mfma_f32_16x16x32_f16 v[64:67], v[80:83], v[12:15], v[64:67]\n" \
  "v_mfma_f32_16x16x32_f16 v[68:71], v[84:87], v[12:15], v[68:71]\n" \
  "v_mfma_f32_16x16x32_f16 v[72:75], v[88:91], v[12:15], v[72:75]\n" \
  "v_mfma_f32_16x16x32_f16 v[76:79], v[92:95], v[12:15], v[76:79]\n" \
  "ds_read_b128 v[24:27], v137 offset:384\n" \
  "ds_read_b128 v[80:83], v138 offset:384\n" \
  "ds_read_b128 v[84:87], v139 offset:384\n" \
  "ds_read_b128 v[88:91], v140 offset:384\n" \
  "ds_read_b128 v[92:95], v141 offset:384\n" \
  "s_waitcnt lgkmcnt(10)\n" \
  "v_mfma_f32_16x16x32_f16 v[64:67], v[96:99], v[16:19], v[64:67]\n" \
  "v_mfma_f32_16x16x32_f16 v[68:71], v[100:103], v[16:19], v[68:71]\n" \
  "v_mfma_f32_16x16x32_f16 v[72:75], v[104:107], v[16:19], v[72:75]\n" \
  "v_mfma_f32_16x16x32_f16 v[76:79], v[108:111], v[16:19], v[76:79]\n" \
  "ds_read_b128 v[28:31], v137 offset:448\n" \
  "ds_read_b128 v[96:99], v138 offset:448\n" \
  "ds_read_b128 v[100:103], v139 offset:448\n" \
  "ds_read_b128 v[104:107], v140 offset:448\n" \
  "ds_read_b128 v[108:111], v141 offset:448\n" \
  "s_cmp_eq_u32 s59, 0\n" \
  "s_cbranch_scc1 110f\n" \
  "s_waitcnt vmcnt(0)\n" \
  "v_readfirstlane_b32 s54, v63\n" \
  "s_cmp_lg_u32 s54, 31\n" \
  "s_cbranch_scc1 110f\n" \
  "v_lshlrev_b32 v130, 6, v160\n" \
  "v_mov_b64 v[128:129], s[50:51]\n" \
  "s_add_u32 s54, s44, 0\n" \
  "s_mov_b64 exec, 0xff\n" \
  "buffer_store_dwordx2 v[128:129], v130, %[rs], s54 offen sc1\n" \
  "s_mov_b64 exec, -1\n" \
  "110:\n" \
  "s_waitcnt lgkmcnt(10)\n" \
  "v_mfma_f32_16x16x32_f16 v[64:67], v[112:115], v[20:23], v[64:67]\n" \
  "v_mfma_f32_16x16x32_f16 v[68:71], v[116:119], v[20:23], v[68:71]\n" \
  "v_mfma_f32_16x16x32_f16 v[72:75], v[120:123], v[20:23], v[72:75]\n" \
  "v_mfma_f32_16x16x32_f16 v[76:79], v[124:127], v[20:23], v[76:79]\n" \
  "s_mov_b64 exec, 1\n" \
  "ds_add_u32 v163, v157 offset:56\n" \
  "s_mov_b64 exec, -1\n" \
  "s_waitcnt lgkmcnt(6)\n" \
  "v_mfma_f32_16x16x32_f16 v[64:67], v[80:83], v[24:27], v[64:67]\n" \
  "v_mfma_f32_16x16x32_f16 v[68:71], v[84:87], v[24:27], v[68:71]\n" \
  "v_mfma_f32_16x16x32_f16 v[72:75], v[88:91], v[24:27], v[72:75]\n" \
  "v_mfma_f32_16x16x32_f16 v[76:79], v[92:95], v[24:27], v[76:79]\n" \
  "s_waitcnt lgkmcnt(1)\n" \
  "v_mfma_f32_16x16x32_f16 v[64:67], v[96:99], v[28:31], v[64:67]\n" \
  "v_mfma_f32_16x16x32_f16 v[68:71], v[100:103], v[28:31], v[68:71]\n" \
  "v_mfma_f32_16x16x32_f16 v[72:75], v[104:107], v[28:31], v[72:75]\n" \
  "v_mfma_f32_16x16x32_f16 v[76:79], v[108:111], v[28:31], v[76:79]\n" \
  "s_nop 4\n" \
  "v_mul_f32 v64, v148, v64\n" \
  "v_mul_f32 v65, v148, v65\n" \
  "v_mul_f32 v66, v148, v66\n" \
  "v_fma_f32 v67, v148, v67, v135\n" \
  "v_mul_f32 v68, v148, v68\n" \
  "v_mul_f32 v69, v148, v69\n" \
  "v_mul_f32 v70, v148, v70\n" \
  "v_mul_f32 v71, v148, v71\n" \
  "v_mul_f32 v72, v148, v72\n" \
  "v_mul_f32 v73, v148, v73\n" \
  "v_mul_f32 v74, v148, v74\n" \
  "v_mul_f32 v75, v148, v75\n" \
  "v_mul_f32 v76, v148, v76\n" \
  "v_mul_f32 v77, v148, v77\n" \
  "v_mul_f32 v78, v148, v78\n" \
  "v_mul_f32 v79, v148, v79\n" \
  "v_cvt_pk_bf16_f32 v64, v64, v65\n" \
  "v_cvt_pk_bf16_f32 v65, v66, v67\n" \
  "v_cvt_pk_bf16_f32 v66, v68, v69\n" \
  "v_cvt_pk_bf16_f32 v67, v70, v71\n" \
  "v_cvt_pk_bf16_f32 v68, v72, v73\n" \
  "v_cvt_pk_bf16_f32 v69, v74, v75\n" \
  "v_cvt_pk_bf16_f32 v70, v76, v77\n" \
  "v_cvt_pk_bf16_f32 v71, v78, v79\n" \
  SST \
  "s_mov_b32 s55, 0\n" \
  "s_branch 112f\n" \
  "111:\n" \
  "s_sleep 1\n" \
  "s_add_u32 s55, s55, 0x400\n" \
  "s_cbranch_scc1 113f\n" \
  "112:\n" \
  "ds_read_b32 v112, v163 offset:56\n" \
  "s_waitcnt lgkmcnt(0)\n" \
  "v_readfirstlane_b32 s54, v112\n" \
  "s_cmp_lt_u32 s54, 2\n" \
  "s_cbranch_scc1 111b\n" \
  "113:\n" \
  "v_alignbit_b32 v72, v65, v64, 16\n" \
  "ds_write_b64 v145, v[64:65] offset:1536\n" \
  "ds_write_b16 v145, v64 offset:5694\n" \
  "ds_write_b32 v145, v72 offset:5696\n" \
  "ds_write_b16_d16_hi v145, v65 offset:5700\n" \
  "ds_write_b32 v145, v64 offset:9852\n" \
  "ds_write_b32 v145, v65 offset:9856\n" \
  "ds_write_b16 v145, v64 offset:14010\n" \
  "ds_write_b32 v145, v72 offset:14012\n" \
  "ds_write_b16_d16_hi v145, v65 offset:14016\n" \
  "v_alignbit_b32 v72, v67, v66, 16\n" \
  "ds_write_b64 v145, v[66:67] offset:1024\n" \
  "ds_write_b16 v145, v66 offset:5182\n" \
  "ds_write_b32 v145, v72 offset:5184\n" \
  "ds_write_b16_d16_hi v145, v67 offset:5188\n" \
  "ds_write_b32 v145, v66 offset:9340\n" \
  "ds_write_b32 v145, v67 offset:9344\n" \
  "ds_write_b16 v145, v66 offset:13498\n" \
  "ds_write_b32 v145, v72 offset:13500\n" \
  "ds_write_b16_d16_hi v145, v67 offset:13504\n" \
  "v_alignbit_b32 v72, v69, v68, 16\n" \
  "ds_write_b64 v145, v[68:69] offset:512\n" \
  "ds_write_b16 v145, v68 offset:4670\n" \
  "ds_write_b32 v145, v72 offset:4672\n" \
  "ds_write_b16_d16_hi v145, v69 offset:4676\n" \
  "ds_write_b32 v145, v68 offset:8828\n" \
  "ds_write_b32 v145, v69 offset:8832\n" \
  "ds_write_b16 v145, v68 offset:12986\n" \
  "ds_write_b32 v145, v72 offset:12988\n" \
  "ds_write_b16_d16_hi v145, v69 offset:12992\n" \
  "v_alignbit_b32 v72, v71, v70, 16\n" \
  "ds_write_b64 v145, v[70:71] offset:0\n" \
  "ds_write_b16 v145, v70 offset:4158\n" \
  "ds_write_b32 v145, v72 offset:4160\n" \
  "ds_write_b16_d16_hi v145, v71 offset:4164\n" \
  "ds_write_b32 v145, v70 offset:8316\n" \
  "ds_write_b32 v145, v71 offset:8320\n" \
  "ds_write_b16 v145, v70 offset:12474\n" \
  "ds_write_b32 v145, v72 offset:12476\n" \
  "ds_write_b16_d16_hi v145, v71 offset:12480\n" \
  "ds_write_b64 v146, v[150:151]\n" \
  "ds_write_b16 v147, v150\n" \
  SST \
  "buffer_load_dwordx2 v[44:45], v149, %[rs], s37 offen sc1\n" \
  "s_waitcnt vmcnt(0)\n" \
  "s_mov_b32 s55, 0\n" \
  "s_branch 115f\n" \
  "114:\n" \
  "s_sleep 2\n" \
  "s_add_u32 s55, s55, 0x800\n" \
  "s_cbranch_scc1 116f\n" \
  "buffer_load_dwordx2 v[44:45], v149, %[rs], s37 offen sc1\n" \
  "s_waitcnt vmcnt(0)\n" \
  "115:\n" \
  "v_cmp_ne_u64 vcc, s[50:51], v[44:45]\n" \
  "s_cbranch_vccnz 114b\n" \
  "116:\n" \
  "buffer_load_dwordx4 v[32:35], v160, %[rs], s36 offen sc1\n" \
  "buffer_load_dwordx4 v[36:39], v160, %[rs], s36 offen offset:1024 sc1\n" \
  "buffer_load_dwordx4 v[48:51], v160, %[rs], s38 offen sc1\n" \
  "buffer_load_dwordx4 v[52:55], v160, %[rs], s38 offen offset:1024 sc1\n" \
  "v_mov_b64 v[148:149], 0\n" \
  "s_waitcnt vmcnt(2)\n" \
  "ds_write_b128 v142, v[32:35]\n" \
  "ds_write_b128 v142, v[36:39] offset:1024\n" \
  "s_mov_b64 exec, 1\n" \
  "ds_add_u32 v156, v157 offset:0\n" \
  "s_mov_b64 exec, -1\n" \
  "s_waitcnt vmcnt(0)\n" \
  "ds_write_b128 v162, v[48:51]\n" \
  "ds_write_b128 v162, v[52:55] offset:1024\n" \
  "s_mov_b64 exec, 1\n" \
  "ds_add_u32 v163, v157 offset:56\n" \
  "s_mov_b64 exec, -1\n" \
  "v_mfma_f32_32x32x16_bf16 v[64:79], v[148:151], v[148:151], 0\n" \
  "v_mfma_f32_32x32x16_bf16 v[80:95], v[148:151], v[148:151], 0\n" \
  "v_mfma_f32_32x32x16_bf16 v[96:111], v[148:151], v[148:151], 0\n" \
  "s_mov_b32 s55, 0\n" \
  "s_branch 118f\n" \
  "117:\n" \
  "s_sleep 1\n" \
  "s_add_u32 s55, s55, 0x400\n" \
  "s_cbranch_scc1 119f\n" \
  "118:\n" \
  "ds_read_b32 v112, v156 offset:0\n" \
  "s_waitcnt lgkmcnt(0)\n" \
  "v_readfirstlane_b32 s54, v112\n" \
  "s_cmp_lt_u32 s54, 4\n" \
  "s_cbranch_scc1 117b\n" \
  "119:\n" \
  "s_mov_b32 s55, 0\n" \
  "s_branch 121f\n" \
  "120:\n" \
  "s_sleep 1\n" \
  "s_add_u32 s55, s55, 0x400\n" \
  "s_cbranch_scc1 122f\n" \
  "121:\n" \
  "ds_read_b32 v112, v163 offset:56\n" \
  "s_waitcnt lgkmcnt(0)\n" \
  "v_readfirstlane_b32 s54, v112\n" \
  "s_cmp_lt_u32 s54, 4\n" \
  "s_cbranch_scc1 120b\n" \
  "122:\n" \
  "s_mov_b32 s57, 0\n" \
  "s_mov_b32 s58, 8\n" \
  "11:\n" \
  "s_add_u32 s56, s39, s40\n" \
  "s_add_u32 s40, s40, 0x10000\n" \
  "s_and_b32 s40, s40, 0x70000\n" \
  "ds_read_b64 v[144:145], v158 offset:3584\n" \
  "ds_read_b64 v[146:147], v158 offset:3592\n" \
  "ds_read_b128 v[140:143], v159 offset:3584\n" \
  "buffer_load_dwordx4 v[0:3], v160, %[rs], s56 offen offset:0 sc1\n" \
  "ds_read_b128 v[136:139], v159 offset:3072\n" \
  "ds_read_b128 v[132:135], v159 offset:2560\n" \
  "ds_read_b128 v[128:131], v159 offset:2048\n" \
  "buffer_load_dwordx4 v[4:7], v160, %[rs], s56 offen offset:1024 sc1\n" \
  "ds_read_b128 v[124:127], v159 offset:1536\n" \
  "ds_read_b128 v[120:123], v159 offset:1024\n" \
  "ds_read_b128 v[116:119], v159 offset:512\n" \
  "buffer_load_dwordx4 v[8:11], v160, %[rs], s56 offen offset:2048 sc1\n" \
  "ds_read_b128 v[112:115], v159 offset:0\n" \
  "ds_read_b64 v[148:149], v158 offset:3072\n" \
  "ds_read_b64 v[150:151], v158 offset:3080\n" \
  "buffer_load_dwordx4 v[12:15], v160, %[rs], s56 offen offset:3072 sc1\n s_add_u32 s56, s56, 4096\n" \
  "s_waitcnt lgkmcnt(6)\n" \
  "v_mfma_f32_16x16x32_bf16 v[108:111], v[144:147], v[140:143], v[108:111]\n" \
  "v_mfma_f32_16x16x32_bf16 v[104:107], v[144:147], v[136:139], v[104:107]\n" \
  "v_mfma_f32_16x16x32_bf16 v[100:103], v[144:147], v[132:135], v[100:103]\n" \
  "buffer_load_dwordx4 v[16:19], v160, %[rs], s56 offen offset:0 sc1\n" \
  "v_mfma_f32_16x16x32_bf16 v[96:99], v[144:147], v[128:131], v[96:99]\n" \
  "s_waitcnt lgkmcnt(0)\n" \
  "v_mfma_f32_16x16x32_bf16 v[92:95], v[144:147], v[124:127], v[92:95]\n" \
  "v_mfma_f32_16x16x32_bf16 v[88:91], v[144:147], v[120:123], v[88:91]\n" \
  "buffer_load_dwordx4 v[20:23], v160, %[rs], s56 offen offset:1024 sc1\n" \
  "v_mfma_f32_16x16x32_bf16 v[84:87], v[144:147], v[116:119], v[84:87]\n" \
  "v_mfma_f32_16x16x32_bf16 v[80:83], v[144:147], v[112:115], v[80:83]\n" \
  "ds_read_b64 v[144:145], v158 offset:2560\n" \
  "buffer_load_dwordx4 v[24:27], v160, %[rs], s56 offen offset:2048 sc1\n" \
  "ds_read_b64 v[146:147], v158 offset:2568\n" \
  "ds_read_b64 v[140:141], v158 offset:2048\n" \
  "ds_read_b64 v[142:143], v158 offset:2056\n" \
  "buffer_load_dwordx4 v[28:31], v160, %[rs], s56 offen offset:3072 sc1\n s_add_u32 s56, s56, 4096\n" \
  "v_mfma_f32_16x16x32_bf16 v[108:111], v[148:151], v[136:139], v[108:111]\n" \
  "v_mfma_f32_16x16x32_bf16 v[104:107], v[148:151], v[132:135], v[104:107]\n" \
  "v_mfma_f32_16x16x32_bf16 v[100:103], v[148:151], v[128:131], v[100:103]\n" \
  "buffer_load_dwordx4 v[32:35], v160, %[rs], s56 offen offset:0 sc1\n" \
  "v_mfma_f32_16x16x32_bf16 v[96:99], v[148:151], v[124:127], v[96:99]\n" \
  "v_mfma_f32_16x16x32_bf16 v[92:95], v[148:151], v[120:123], v[92:95]\n" \
  "v_mfma_f32_16x16x32_bf16 v[88:91], v[148:151], v[116:119], v[88:91]\n" \
  "buffer_load_dwordx4 v[36:39], v160, %[rs], s56 offen offset:1024 sc1\n" \
  "v_mfma_f32_16x16x32_bf16 v[84:87], v[148:151], v[112:115], v[84:87]\n" \
  "ds_read_b64 v[148:149], v158 offset:1536\n" \
  "ds_read_b64 v[150:151], v158 offset:1544\n" \
  "buffer_load_dwordx4 v[40:43], v160, %[rs], s56 offen offset:2048 sc1\n" \
  "ds_read_b64 v[136:137], v158 offset:1024\n" \
  "ds_read_b64 v[138:139], v158 offset:1032\n" \
  "s_waitcnt lgkmcnt(4)\n" \
  "v_mfma_f32_16x16x32_bf16 v[108:111], v[144:147], v[132:135], v[108:111]\n" \
  "buffer_load_dwordx4 v[44:47], v160, %[rs], s56 offen offset:3072 sc1\n s_add_u32 s56, s56, 4096\n" \
  "v_mfma_f32_16x16x32_bf16 v[104:107], v[144:147], v[128:131], v[104:107]\n" \
  "v_mfma_f32_16x16x32_bf16 v[100:103], v[144:147], v[124:127], v[100:103]\n" \
  "v_mfma_f32_16x16x32_bf16 v[96:99], v[144:147], v[120:123], v[96:99]\n" \
  "buffer_load_dwordx4 v[48:51], v160, %[rs], s56 offen offset:0 sc1\n" \
  "v_mfma_f32_16x16x32_bf16 v[92:95], v[144:147], v[116:119], v[92:95]\n" \
  "v_mfma_f32_16x16x32_bf16 v[88:91], v[144:147], v[112:115], v[88:91]\n" \
  "ds_read_b64 v[144:145], v158 offset:512\n" \
  "buffer_load_dwordx4 v[52:55], v160, %[rs], s56 offen offset:1024 sc1\n" \
  "ds_read_b64 v[146:147], v158 offset:520\n" \
  "ds_read_b64 v[132:133], v158 offset:0\n" \
  "ds_read_b64 v[134:135], v158 offset:8\n" \
  "buffer_load_dwordx4 v[56:59], v160, %[rs], s56 offen offset:2048 sc1\n" \
  "v_mfma_f32_16x16x32_bf16 v[108:111], v[140:143], v[128:131], v[108:111]\n" \
  "v_mfma_f32_16x16x32_bf16 v[104:107], v[140:143], v[124:127], v[104:107]\n" \
  "v_mfma_f32_16x16x32_bf16 v[100:103], v[140:143], v[120:123], v[100:103]\n" \
  "buffer_load_dwordx4 v[60:63], v160, %[rs], s56 offen offset:3072 sc1\n" \
  "v_mfma_f32_16x16x32_bf16 v[96:99], v[140:143], v[116:119], v[96:99]\n" \
  "v_mfma_f32_16x16x32_bf16 v[92:95], v[140:143], v[112:115], v[92:95]\n" \
  "v_subrev_u32 v158, 64, v158\n" \
  "v_subrev_u32 v159, 64, v159\n" \
  "s_waitcnt lgkmcnt(4)\n" \
  "v_mfma_f32_16x16x32_bf16 v[108:111], v[148:151], v[124:127], v[108:111]\n" \
  "v_mfma_f32_16x16x32_bf16 v[104:107], v[148:151], v[120:123], v[104:107]\n" \
  "v_mfma_f32_16x16x32_bf16 v[100:103], v[148:151], v[116:119], v[100:103]\n" \
  "v_mfma_f32_16x16x32_bf16 v[96:99], v[148:151], v[112:115], v[96:99]\n" \
  "v_mfma_f32_16x16x32_bf16 v[108:111], v[136:139], v[120:123], v[108:111]\n" \
  "v_mfma_f32_16x16x32_bf16 v[104:107], v[136:139], v[116:119], v[104:107]\n" \
  "v_mfma_f32_16x16x32_bf16 v[100:103], v[136:139], v[112:115], v[100:103]\n" \
  "s_waitcnt lgkmcnt(0)\n" \
  "v_mfma_f32_16x16x32_bf16 v[108:111], v[144:147], v[116:119], v[108:111]\n" \
  "v_mfma_f32_16x16x32_bf16 v[104:107], v[144:147], v[112:115], v[104:107]\n" \
  "v_mfma_f32_16x16x32_bf16 v[108:111], v[132:135], v[112:115], v[108:111]\n" \
  SST \
  "s_cmp_eq_u32 s57, 3\n" \
  "s_cbranch_scc1 12f\n" \
  "s_lshl_b32 s54, s57, 2\n" \
  "v_add_u32 v113, s54, v156\n" \
  "s_lshl_b32 s54, s57, 14\n" \
  "v_add_u32 v114, s54, v152\n" \
  "s_lshl_b32 s54, s57, 11\n" \
  "v_add_u32 v115, s54, v153\n" \
  "s_mov_b32 s55, 0\n" \
  "s_branch 124f\n" \
  "123:\n" \
  "s_sleep 5\n" \
  "s_add_u32 s55, s55, 0x400\n" \
  "s_cbranch_scc1 125f\n" \
  "124:\n" \
  "ds_read_b32 v112, v113 offset:96\n" \
  "s_waitcnt lgkmcnt(0)\n" \
  "v_readfirstlane_b32 s54, v112\n" \
  "s_cmp_lt_u32 s54, 8\n" \
  "s_cbranch_scc1 123b\n" \
  "125:\n" \
  SST \
  "ds_read2st64_b64 v[120:123], v114 offset0:0 offset1:4\n" \
  "ds_read2st64_b64 v[124:127], v114 offset0:8 offset1:12\n" \
  "ds_read2st64_b64 v[128:131], v114 offset0:16 offset1:20\n" \
  "ds_read2st64_b64 v[132:135], v114 offset0:24 offset1:28\n" \
  "ds_read_b128 v[136:139], v115 offset:0\n" \
  "ds_read_b128 v[140:143], v115 offset:16\n" \
  "s_waitcnt lgkmcnt(4)\n" \
  "v_add_f32 v120, v120, v122\n" \
  "v_add_f32 v121, v121, v123\n" \
  "v_add_f32 v124, v124, v126\n" \
  "v_add_f32 v125, v125, v127\n" \
  "s_waitcnt lgkmcnt(2)\n" \
  "v_add_f32 v128, v128, v130\n" \
  "v_add_f32 v129, v129, v131\n" \
  "v_add_f32 v132, v132, v134\n" \
  "v_add_f32 v133, v133, v135\n" \
  "v_add_f32 v120, v120, v124\n" \
  "v_add_f32 v121, v121, v125\n" \
  "s_waitcnt lgkmcnt(0)\n" \
  "v_add_f32 v136, v136, v138\n" \
  "v_add_f32 v137, v137, v139\n" \
  "v_add_f32 v140, v140, v142\n" \
  "v_add_f32 v141, v141, v143\n" \
  "v_add_f32 v136, v136, v140\n" \
  "v_add_f32 v137, v137, v141\n" \
  "v_add_f32 v136, v136, v137\n" \
  "v_rcp_f32 v137, v136\n" \
  "v_add_f32 v128, v128, v132\n" \
  "v_add_f32 v129, v129, v133\n" \
  "v_add_f32 v120, v120, v128\n" \
  "v_add_f32 v121, v121, v129\n" \
  "v_mul_f32 v120, v120, v137\n" \
  "v_mul_f32 v121, v121, v137\n" \
  "v_cvt_pk_bf16_f32 v122, v120, v121\n" \
  "ds_write_b32 v154, v122 offset:0\n" \
  "s_mov_b64 exec, 1\n" \
  "ds_add_u32 v156, v157 offset:0\n" \
  "s_mov_b64 exec, -1\n" \
  "v_max3_f32 v166, v166, v136, v137\n" \
  "v_xor_b32 v154, 0x800, v154\n" \
  "s_mov_b32 s55, 0\n" \
  "s_branch 127f\n" \
  "126:\n" \
  "s_sleep 1\n" \
  "s_add_u32 s55, s55, 0x400\n" \
  "s_cbranch_scc1 128f\n" \
  "127:\n" \
  "ds_read_b32 v112, v156 offset:0\n" \
  "s_waitcnt lgkmcnt(0)\n" \
  "v_readfirstlane_b32 s54, v112\n" \
  "s_cmp_lt_u32 s54, s58\n" \
  "s_cbranch_scc1 126b\n" \
  "128:\n" \
  "ds_read_b128 v[112:115], v155 offset:0\n" \
  "ds_read_b128 v[116:119], v155 offset:64\n" \
  "v_xor_b32 v155, 0x800, v155\n" \
  "s_waitcnt vmcnt(0) lgkmcnt(0)\n" \
  "v_smfmac_f32_16x16x64_bf16 v[64:67], v[112:115], v[0:7], v161\n" \
  "v_smfmac_f32_16x16x64_bf16 v[68:71], v[112:115], v[8:15], v161\n" \
  "v_smfmac_f32_16x16x64_bf16 v[72:75], v[112:115], v[16:23], v161\n" \
  "v_smfmac_f32_16x16x64_bf16 v[76:79], v[112:115], v[24:31], v161\n" \
  "v_smfmac_f32_16x16x64_bf16 v[64:67], v[116:119], v[32:39], v161\n" \
  "v_smfmac_f32_16x16x64_bf16 v[68:71], v[116:119], v[40:47], v161\n" \
  "v_smfmac_f32_16x16x64_bf16 v[72:75], v[116:119], v[48:55], v161\n" \
  "v_smfmac_f32_16x16x64_bf16 v[76:79], v[116:119], v[56:63], v161\n" \
  "s_add_u32 s57, s57, 1\n" \
  "s_add_u32 s58, s58, 4\n" \
  "s_branch 11b\n" \
  "12:\n" \
  "v_xor_b32 v154, 0x800, v154\n" \
  "v_xor_b32 v155, 0x800, v155\n" \
  "s_nop 1\n" \
  "v_add_u32 v148, %[tab], v160\n" \
  "v_add_u32 v149, s42, v160\n" \
  "ds_read_b64 v[150:151], v148 offset:2048\n" \
  "s_cmp_lg_u32 s49, 0\n" \
  "s_cbranch_scc1 13f\n" \
  "ds_write_b128 v162, v[96:99] offset:4096\n" \
  "ds_write_b128 v162, v[100:103] offset:5120\n" \
  "ds_write_b128 v162, v[104:107] offset:6144\n" \
  "ds_write_b128 v162, v[108:111] offset:7168\n" \
  "s_mov_b64 exec, 1\n" \
  "ds_add_u32 v163, v157 offset:56\n" \
  "s_mov_b64 exec, -1\n" \
  "s_mov_b32 s55, 0\n" \
  "s_branch 130f\n" \
  "129:\n" \
  "s_sleep 1\n" \
  "s_add_u32 s55, s55, 0x400\n" \
  "s_cbranch_scc1 131f\n" \
  "130:\n" \
  "ds_read_b32 v112, v163 offset:56\n" \
  "s_waitcnt lgkmcnt(0)\n" \
  "v_readfirstlane_b32 s54, v112\n" \
  "s_cmp_lt_u32 s54, 6\n" \
  "s_cbranch_scc1 129b\n" \
  "131:\n" \
  "ds_read_b128 v[112:115], v162 offset:8192\n" \
  "ds_read_b128 v[116:119], v162 offset:9216\n" \
  "ds_read_b128 v[120:123], v162 offset:10240\n" \
  "ds_read_b128 v[124:127], v162 offset:11264\n" \
  "s_waitcnt lgkmcnt(3)\n" \
  "v_add_f32 v112, v112, v80\n" \
  "v_add_f32 v113, v113, v81\n" \
  "v_add_f32 v114, v114, v82\n" \
  "v_add_f32 v115, v115, v83\n" \
  "s_waitcnt lgkmcnt(2)\n" \
  "v_add_f32 v116, v116, v84\n" \
  "v_add_f32 v117, v117, v85\n" \
  "v_add_f32 v118, v118, v86\n" \
  "v_add_f32 v119, v119, v87\n" \
  "s_waitcnt lgkmcnt(1)\n" \
  "v_add_f32 v120, v120, v88\n" \
  "v_add_f32 v121, v121, v89\n" \
  "v_add_f32 v122, v122, v90\n" \
  "v_add_f32 v123, v123, v91\n" \
  "s_waitcnt lgkmcnt(0)\n" \
  "v_add_f32 v124, v124, v92\n" \
  "v_add_f32 v125, v125, v93\n" \
  "v_add_f32 v126, v126, v94\n" \
  "v_add_f32 v127, v127, v95\n" \
  "s_branch 14f\n" \
  "13:\n" \
  "ds_write_b128 v162, v[80:83] offset:6144\n" \
  "ds_write_b128 v162, v[84:87] offset:7168\n" \
  "ds_write_b128 v162, v[88:91] offset:8192\n" \
  "ds_write_b128 v162, v[92:95] offset:9216\n" \
  "s_mov_b64 exec, 1\n" \
  "ds_add_u32 v163, v157 offset:56\n" \
  "s_mov_b64 exec, -1\n" \
  "s_mov_b32 s55, 0\n" \
  "s_branch 133f\n" \
  "132:\n" \
  "s_sleep 1\n" \
  "s_add_u32 s55, s55, 0x400\n" \
  "s_cbranch_scc1 134f\n" \
  "133:\n" \
  "ds_read_b32 v112, v163 offset:56\n" \
  "s_waitcnt lgkmcnt(0)\n" \
  "v_readfirstlane_b32 s54, v112\n" \
  "s_cmp_lt_u32 s54, 6\n" \
  "s_cbranch_scc1 132b\n" \
  "134:\n" \
  "ds_read_b128 v[112:115], v162 offset:2048\n" \
  "ds_read_b128 v[116:119], v162 offset:3072\n" \
  "ds_read_b128 v[120:123], v162 offset:4096\n" \
  "ds_read_b128 v[124:127], v162 offset:5120\n" \
  "s_waitcnt lgkmcnt(3)\n" \
  "v_add_f32 v112, v112, v96\n" \
  "v_add_f32 v113, v113, v97\n" \
  "v_add_f32 v114, v114, v98\n" \
  "v_add_f32 v115, v115, v99\n" \
  "s_waitcnt lgkmcnt(2)\n" \
  "v_add_f32 v116, v116, v100\n" \
  "v_add_f32 v117, v117, v101\n" \
  "v_add_f32 v118, v118, v102\n" \
  "v_add_f32 v119, v119, v103\n" \
  "s_waitcnt lgkmcnt(1)\n" \
  "v_add_f32 v120, v120, v104\n" \
  "v_add_f32 v121, v121, v105\n" \
  "v_add_f32 v122, v122, v106\n" \
  "v_add_f32 v123, v123, v107\n" \
  "s_waitcnt lgkmcnt(0)\n" \
  "v_add_f32 v124, v124, v108\n" \
  "v_add_f32 v125, v125, v109\n" \
  "v_add_f32 v126, v126, v110\n" \
  "v_add_f32 v127, v127, v111\n" \
  "14:\n" \
  "v_cvt_pk_bf16_f32 v112, v112, v113\n" \
  "v_cvt_pk_bf16_f32 v113, v114, v115\n" \
  "v_cvt_pk_bf16_f32 v114, v116, v117\n" \
  "v_cvt_pk_bf16_f32 v115, v118, v119\n" \
  "v_cvt_pk_bf16_f32 v116, v120, v121\n" \
  "v_cvt_pk_bf16_f32 v117, v122, v123\n" \
  "v_cvt_pk_bf16_f32 v118, v124, v125\n" \
  "v_cvt_pk_bf16_f32 v119, v126, v127\n" \
  "v_add_u32 v150, s42, v150\n" \
  "ds_read_b128 v[104:107], v148\n" \
  "ds_read_b128 v[108:111], v148 offset:1024\n" \
  "ds_write2st64_b64 v150, v[112:113], v[114:115] offset1:1\n" \
  "ds_write2st64_b64 v150, v[116:117], v[118:119] offset0:2 offset1:3\n" \
  "ds_read_b128 v[120:123], v149\n" \
  "ds_read_b128 v[124:127], v149 offset:1024\n" \
  "s_waitcnt lgkmcnt(1)\n" \
  "buffer_store_dwordx4 v[120:123], v160, %[rs], s41 offen sc1\n" \
  "s_waitcnt lgkmcnt(0)\n" \
  "buffer_store_dwordx4 v[124:127], v160, %[rs], s41 offen offset:1024 sc1\n" \
  "s_waitcnt vmcnt(0)\n" \
  "s_mov_b64 exec, 1\n" \
  "ds_add_rtn_u32 v144, v156, v157 offset:12\n" \
  "s_waitcnt lgkmcnt(0)\n" \
  "v_readfirstlane_b32 s54, v144\n" \
  "v_mov_b32 v144, 1\n" \
  "s_cmp_eq_u32 s54, 7\n" \
  "s_cselect_b32 s59, 1, 0\n" \
  "s_mov_b32 exec_lo, s59\n" \
  "buffer_atomic_add v144, off, %[rs], s43 offset:512 sc0\n" \
  "s_mov_b64 exec, -1\n" \
  "v_add_u32 v109, s42, v109\n" \
  "v_add_u32 v110, s42, v110\n" \
  "s_cmp_eq_u32 s59, 0\n" \
  "s_cbranch_scc1 135f\n" \
  "s_waitcnt vmcnt(0)\n" \
  "v_readfirstlane_b32 s54, v144\n" \
  "s_cmp_lg_u32 s54, 31\n" \
  "s_cbranch_scc1 135f\n" \
  "v_lshlrev_b32 v148, 6, v160\n" \
  "v_mov_b64 v[146:147], s[50:51]\n" \
  "s_add_u32 s54, s44, 8192\n" \
  "s_mov_b64 exec, 0xff\n" \
  "buffer_store_dwordx2 v[146:147], v148, %[rs], s54 offen sc1\n" \
  "s_mov_b64 exec, -1\n" \
  "135:\n" \
  SST \
  "s_mov_b32 s55, 0\n" \
  "s_branch 137f\n" \
  "136:\n" \
  "s_sleep 5\n" \
  "s_add_u32 s55, s55, 0x400\n" \
  "s_cbranch_scc1 138f\n" \
  "137:\n" \
  "ds_read_b32 v112, v156 offset:108\n" \
  "s_waitcnt lgkmcnt(0)\n" \
  "v_readfirstlane_b32 s54, v112\n" \
  "s_cmp_lt_u32 s54, 8\n" \
  "s_cbranch_scc1 136b\n" \
  "138:\n" \
  SST \
  "ds_read2st64_b64 v[120:123], v152 offset0:96 offset1:100\n" \
  "ds_read2st64_b64 v[124:127], v152 offset0:104 offset1:108\n" \
  "ds_read2st64_b64 v[128:131], v152 offset0:112 offset1:116\n" \
  "ds_read2st64_b64 v[132:135], v152 offset0:120 offset1:124\n" \
  "ds_read_b128 v[136:139], v153 offset:6144\n" \
  "ds_read_b128 v[140:143], v153 offset:6160\n" \
  "s_waitcnt lgkmcnt(4)\n" \
  "v_add_f32 v120, v120, v122\n" \
  "v_add_f32 v121, v121, v123\n" \
  "v_add_f32 v124, v124, v126\n" \
  "v_add_f32 v125, v125, v127\n" \
  "s_waitcnt lgkmcnt(2)\n" \
  "v_add_f32 v128, v128, v130\n" \
  "v_add_f32 v129, v129, v131\n" \
  "v_add_f32 v132, v132, v134\n" \
  "v_add_f32 v133, v133, v135\n" \
  "v_add_f32 v120, v120, v124\n" \
  "v_add_f32 v121, v121, v125\n" \
  "s_waitcnt lgkmcnt(0)\n" \
  "v_add_f32 v136, v136, v138\n" \
  "v_add_f32 v137, v137, v139\n" \
  "v_add_f32 v140, v140, v142\n" \
  "v_add_f32 v141, v141, v143\n" \
  "v_add_f32 v136, v136, v140\n" \
  "v_add_f32 v137, v137, v141\n" \
  "v_add_f32 v136, v136, v137\n" \
  "v_rcp_f32 v137, v136\n" \
  "v_add_f32 v128, v128, v132\n" \
  "v_add_f32 v129, v129, v133\n" \
  "v_add_f32 v120, v120, v128\n" \
  "v_add_f32 v121, v121, v129\n" \
  "v_mul_f32 v120, v120, v137\n" \
  "v_mul_f32 v121, v121, v137\n" \
  "v_cvt_pk_bf16_f32 v122, v120, v121\n" \
  "ds_write_b32 v154, v122 offset:2048\n" \
  "s_mov_b64 exec, 1\n" \
  "ds_add_u32 v156, v157 offset:0\n" \
  "s_mov_b64 exec, -1\n" \
  "v_max3_f32 v166, v166, v136, v137\n" \
  "s_mov_b32 s55, 0\n" \
  "s_branch 140f\n" \
  "139:\n" \
  "s_sleep 1\n" \
  "s_add_u32 s55, s55, 0x400\n" \
  "s_cbranch_scc1 141f\n" \
  "140:\n" \
  "ds_read_b32 v112, v156 offset:0\n" \
  "s_waitcnt lgkmcnt(0)\n" \
  "v_readfirstlane_b32 s54, v112\n" \
  "s_cmp_lt_u32 s54, 20\n" \
  "s_cbranch_scc1 139b\n" \
  "141:\n" \
  "ds_read_b128 v[112:115], v155 offset:2048\n" \
  "ds_read_b128 v[116:119], v155 offset:2112\n" \
  "s_waitcnt vmcnt(0) lgkmcnt(0)\n" \
  "v_smfmac_f32_16x16x64_bf16 v[64:67], v[112:115], v[0:7], v161\n" \
  "v_smfmac_f32_16x16x64_bf16 v[68:71], v[112:115], v[8:15], v161\n" \
  "v_smfmac_f32_16x16x64_bf16 v[72:75], v[112:115], v[16:23], v161\n" \
  "v_smfmac_f32_16x16x64_bf16 v[76:79], v[112:115], v[24:31], v161\n" \
  "v_smfmac_f32_16x16x64_bf16 v[64:67], v[116:119], v[32:39], v161\n" \
  "v_smfmac_f32_16x16x64_bf16 v[68:71], v[116:119], v[40:47], v161\n" \
  "v_smfmac_f32_16x16x64_bf16 v[72:75], v[116:119], v[48:55], v161\n" \
  "v_smfmac_f32_16x16x64_bf16 v[76:79], v[116:119], v[56:63], v161\n" \
  "s_add_u32 s56, s39, s40\n" \
  "s_add_u32 s40, s40, 0x10000\n" \
  "s_and_b32 s40, s40, 0x70000\n" \
  "buffer_load_dwordx4 v[0:3], v160, %[rs], s56 offen offset:0 sc1\n" \
  "buffer_load_dwordx4 v[4:7], v160, %[rs], s56 offen offset:1024 sc1\n" \
  "buffer_load_dwordx4 v[8:11], v160, %[rs], s56 offen offset:2048 sc1\n" \
  "buffer_load_dwordx4 v[12:15], v160, %[rs], s56 offen offset:3072 sc1\n s_add_u32 s56, s56, 4096\n" \
  "buffer_load_dwordx4 v[16:19], v160, %[rs], s56 offen offset:0 sc1\n" \
  "buffer_load_dwordx4 v[20:23], v160, %[rs], s56 offen offset:1024 sc1\n" \
  "buffer_load_dwordx4 v[24:27], v160, %[rs], s56 offen offset:2048 sc1\n" \
  "buffer_load_dwordx4 v[28:31], v160, %[rs], s56 offen offset:3072 sc1\n s_add_u32 s56, s56, 4096\n" \
  "buffer_load_dwordx4 v[32:35], v160, %[rs], s56 offen offset:0 sc1\n" \
  "buffer_load_dwordx4 v[36:39], v160, %[rs], s56 offen offset:1024 sc1\n" \
  "buffer_load_dwordx4 v[40:43], v160, %[rs], s56 offen offset:2048 sc1\n" \
  "buffer_load_dwordx4 v[44:47], v160, %[rs], s56 offen offset:3072 sc1\n s_add_u32 s56, s56, 4096\n" \
  "buffer_load_dwordx4 v[48:51], v160, %[rs], s56 offen offset:0 sc1\n" \
  "buffer_load_dwordx4 v[52:55], v160, %[rs], s56 offen offset:1024 sc1\n" \
  "buffer_load_dwordx4 v[56:59], v160, %[rs], s56 offen offset:2048 sc1\n" \
  "buffer_load_dwordx4 v[60:63], v160, %[rs], s56 offen offset:3072 sc1\n" \
  "s_mov_b32 s55, 0\n" \
  "s_branch 143f\n" \
  "142:\n" \
  "s_sleep 5\n" \
  "s_add_u32 s55, s55, 0x400\n" \
  "s_cbranch_scc1 144f\n" \
  "143:\n" \
  "ds_read_b32 v112, v156 offset:96\n" \
  "s_waitcnt lgkmcnt(0)\n" \
  "v_readfirstlane_b32 s54, v112\n" \
  "s_cmp_lt_u32 s54, 16\n" \
  "s_cbranch_scc1 142b\n" \
  "144:\n" \
  SST \
  "ds_read2st64_b64 v[120:123], v152 offset0:0 offset1:4\n" \
  "ds_read2st64_b64 v[124:127], v152 offset0:8 offset1:12\n" \
  "ds_read2st64_b64 v[128:131], v152 offset0:16 offset1:20\n" \
  "ds_read2st64_b64 v[132:135], v152 offset0:24 offset1:28\n" \
  "ds_read_b128 v[136:139], v153 offset:0\n" \
  "ds_read_b128 v[140:143], v153 offset:16\n" \
  "s_waitcnt lgkmcnt(4)\n" \
  "v_add_f32 v120, v120, v122\n" \
  "v_add_f32 v121, v121, v123\n" \
  "v_add_f32 v124, v124, v126\n" \
  "v_add_f32 v125, v125, v127\n" \
  "s_waitcnt lgkmcnt(2)\n" \
  "v_add_f32 v128, v128, v130\n" \
  "v_add_f32 v129, v129, v131\n" \
  "v_add_f32 v132, v132, v134\n" \
  "v_add_f32 v133, v133, v135\n" \
  "v_add_f32 v120, v120, v124\n" \
  "v_add_f32 v121, v121, v125\n" \
  "s_waitcnt lgkmcnt(0)\n" \
  "v_add_f32 v136, v136, v138\n" \
  "v_add_f32 v137, v137, v139\n" \
  "v_add_f32 v140, v140, v142\n" \
  "v_add_f32 v141, v141, v143\n" \
  "v_add_f32 v136, v136, v140\n" \
  "v_add_f32 v137, v137, v141\n" \
  "v_add_f32 v136, v136, v137\n" \
  "v_rcp_f32 v137, v136\n" \
  "v_add_f32 v128, v128, v132\n" \
  "v_add_f32 v129, v129, v133\n" \
  "v_add_f32 v120, v120, v128\n" \
  "v_add_f32 v121, v121, v129\n" \
  "v_mul_f32 v120, v120, v137\n" \
  "v_mul_f32 v121, v121, v137\n" \
  "v_cvt_pk_bf16_f32 v122, v120, v121\n" \
  "ds_write_b32 v154, v122 offset:0\n" \
  "s_mov_b64 exec, 1\n" \
  "ds_add_u32 v156, v157 offset:0\n" \
  "s_mov_b64 exec, -1\n" \
  "v_max3_f32 v166, v166, v136, v137\n" \
  "s_mov_b32 s55, 0\n" \
  "s_branch 146f\n" \
  "145:\n" \
  "s_sleep 1\n" \
  "s_add_u32 s55, s55, 0x400\n" \
  "s_cbranch_scc1 147f\n" \
  "146:\n" \
  "ds_read_b32 v112, v156 offset:0\n" \
  "s_waitcnt lgkmcnt(0)\n" \
  "v_readfirstlane_b32 s54, v112\n" \
  "s_cmp_lt_u32 s54, 24\n" \
  "s_cbranch_scc1 145b\n" \
  "147:\n" \
  "ds_read_b128 v[112:115], v155 offset:0\n" \
  "ds_read_b128 v[116:119], v155 offset:64\n" \
  "s_waitcnt vmcnt(0) lgkmcnt(0)\n" \
  "v_smfmac_f32_16x16x64_bf16 v[64:67], v[112:115], v[0:7], v161\n" \
  "v_smfmac_f32_16x16x64_bf16 v[68:71], v[112:115], v[8:15], v161\n" \
  "v_smfmac_f32_16x16x64_bf16 v[72:75], v[112:115], v[16:23], v161\n" \
  "v_smfmac_f32_16x16x64_bf16 v[76:79], v[112:115], v[24:31], v161\n" \
  "v_smfmac_f32_16x16x64_bf16 v[64:67], v[116:119], v[32:39], v161\n" \
  "v_smfmac_f32_16x16x64_bf16 v[68:71], v[116:119], v[40:47], v161\n" \
  "v_smfmac_f32_16x16x64_bf16 v[72:75], v[116:119], v[48:55], v161\n" \
  "v_smfmac_f32_16x16x64_bf16 v[76:79], v[116:119], v[56:63], v161\n" \
  "s_add_u32 s56, s39, s40\n" \
  "s_add_u32 s40, s40, 0x10000\n" \
  "s_and_b32 s40, s40, 0x70000\n" \
  "buffer_load_dwordx4 v[0:3], v160, %[rs], s56 offen offset:0 sc1\n" \
  "buffer_load_dwordx4 v[4:7], v160, %[rs], s56 offen offset:1024 sc1\n" \
  "buffer_load_dwordx4 v[8:11], v160, %[rs], s56 offen offset:2048 sc1\n" \
  "buffer_load_dwordx4 v[12:15], v160, %[rs], s56 offen offset:3072 sc1\n s_add_u32 s56, s56, 4096\n" \
  "buffer_load_dwordx4 v[16:19], v160, %[rs], s56 offen offset:0 sc1\n" \
  "buffer_load_dwordx4 v[20:23], v160, %[rs], s56 offen offset:1024 sc1\n" \
  "buffer_load_dwordx4 v[24:27], v160, %[rs], s56 offen offset:2048 sc1\n" \
  "buffer_load_dwordx4 v[28:31], v160, %[rs], s56 offen offset:3072 sc1\n s_add_u32 s56, s56, 4096\n" \
  "buffer_load_dwordx4 v[32:35], v160, %[rs], s56 offen offset:0 sc1\n" \
  "buffer_load_dwordx4 v[36:39], v160, %[rs], s56 offen offset:1024 sc1\n" \
  "buffer_load_dwordx4 v[40:43], v160, %[rs], s56 offen offset:2048 sc1\n" \
  "buffer_load_dwordx4 v[44:47], v160, %[rs], s56 offen offset:3072 sc1\n s_add_u32 s56, s56, 4096\n" \
  "buffer_load_dwordx4 v[48:51], v160, %[rs], s56 offen offset:0 sc1\n" \
  "buffer_load_dwordx4 v[52:55], v160, %[rs], s56 offen offset:1024 sc1\n" \
  "buffer_load_dwordx4 v[56:59], v160, %[rs], s56 offen offset:2048 sc1\n" \
  "buffer_load_dwordx4 v[60:63], v160, %[rs], s56 offen offset:3072 sc1\n" \
  "buffer_load_dwordx2 v[144:145], v151, %[rs], s45 offen sc1\n" \
  "s_mov_b32 s55, 0\n" \
  "s_branch 149f\n" \
  "148:\n" \
  "s_sleep 5\n" \
  "s_add_u32 s55, s55, 0x400\n" \
  "s_cbranch_scc1 150f\n" \
  "149:\n" \
  "ds_read_b32 v112, v156 offset:100\n" \
  "s_waitcnt lgkmcnt(0)\n" \
  "v_readfirstlane_b32 s54, v112\n" \
  "s_cmp_lt_u32 s54, 16\n" \
  "s_cbranch_scc1 148b\n" \
  "150:\n" \
  SST \
  "ds_read2st64_b64 v[120:123], v152 offset0:32 offset1:36\n" \
  "ds_read2st64_b64 v[124:127], v152 offset0:40 offset1:44\n" \
  "ds_read2st64_b64 v[128:131], v152 offset0:48 offset1:52\n" \
  "ds_read2st64_b64 v[132:135], v152 offset0:56 offset1:60\n" \
  "ds_read_b128 v[136:139], v153 offset:2048\n" \
  "ds_read_b128 v[140:143], v153 offset:2064\n" \
  "s_waitcnt lgkmcnt(4)\n" \
  "v_add_f32 v120, v120, v122\n" \
  "v_add_f32 v121, v121, v123\n" \
  "v_add_f32 v124, v124, v126\n" \
  "v_add_f32 v125, v125, v127\n" \
  "s_waitcnt lgkmcnt(2)\n" \
  "v_add_f32 v128, v128, v130\n" \
  "v_add_f32 v129, v129, v131\n" \
  "v_add_f32 v132, v132, v134\n" \
  "v_add_f32 v133, v133, v135\n" \
  "v_add_f32 v120, v120, v124\n" \
  "v_add_f32 v121, v121, v125\n" \
  "s_waitcnt lgkmcnt(0)\n" \
  "v_add_f32 v136, v136, v138\n" \
  "v_add_f32 v137, v137, v139\n" \
  "v_add_f32 v140, v140, v142\n" \
  "v_add_f32 v141, v141, v143\n" \
  "v_add_f32 v136, v136, v140\n" \
  "v_add_f32 v137, v137, v141\n" \
  "v_add_f32 v136, v136, v137\n" \
  "v_rcp_f32 v137, v136\n" \
  "v_add_f32 v128, v128, v132\n" \
  "v_add_f32 v129, v129, v133\n" \
  "v_add_f32 v120, v120, v128\n" \
  "v_add_f32 v121, v121, v129\n" \
  "v_mul_f32 v120, v120, v137\n" \
  "v_mul_f32 v121, v121, v137\n" \
  "v_cvt_pk_bf16_f32 v122, v120, v121\n" \
  "ds_write_b32 v154, v122 offset:2048\n" \
  "s_mov_b64 exec, 1\n" \
  "ds_add_u32 v156, v157 offset:0\n" \
  "s_mov_b64 exec, -1\n" \
  "v_max3_f32 v166, v166, v136, v137\n" \
  "s_mov_b32 s55, 0\n" \
  "s_branch 152f\n" \
  "151:\n" \
  "s_sleep 1\n" \
  "s_add_u32 s55, s55, 0x400\n" \
  "s_cbranch_scc1 153f\n" \
  "152:\n" \
  "ds_read_b32 v112, v156 offset:0\n" \
  "s_waitcnt lgkmcnt(0)\n" \
  "v_readfirstlane_b32 s54, v112\n" \
  "s_cmp_lt_u32 s54, 28\n" \
  "s_cbranch_scc1 151b\n" \
  "153:\n" \
  "ds_read_b128 v[112:115], v155 offset:2048\n" \
  "ds_read_b128 v[116:119], v155 offset:2112\n" \
  "s_waitcnt vmcnt(0) lgkmcnt(0)\n" \
  "v_smfmac_f32_16x16x64_bf16 v[64:67], v[112:115], v[0:7], v161\n" \
  "v_smfmac_f32_16x16x64_bf16 v[68:71], v[112:115], v[8:15], v161\n" \
  "v_smfmac_f32_16x16x64_bf16 v[72:75], v[112:115], v[16:23], v161\n" \
  "v_smfmac_f32_16x16x64_bf16 v[76:79], v[112:115], v[24:31], v161\n" \
  "v_smfmac_f32_16x16x64_bf16 v[64:67], v[116:119], v[32:39], v161\n" \
  "v_smfmac_f32_16x16x64_bf16 v[68:71], v[116:119], v[40:47], v161\n" \
  "v_smfmac_f32_16x16x64_bf16 v[72:75], v[116:119], v[48:55], v161\n" \
  "v_smfmac_f32_16x16x64_bf16 v[76:79], v[116:119], v[56:63], v161\n" \
  "s_add_u32 s56, s39, s40\n" \
  "s_add_u32 s40, s40, 0x10000\n" \
  "s_and_b32 s40, s40, 0x70000\n" \
  "buffer_load_dwordx4 v[0:3], v160, %[rs], s56 offen offset:0 sc1\n" \
  "buffer_load_dwordx4 v[4:7], v160, %[rs], s56 offen offset:1024 sc1\n" \
  "buffer_load_dwordx4 v[8:11], v160, %[rs], s56 offen offset:2048 sc1\n" \
  "buffer_load_dwordx4 v[12:15], v160, %[rs], s56 offen offset:3072 sc1\n s_add_u32 s56, s56, 4096\n" \
  "buffer_load_dwordx4 v[16:19], v160, %[rs], s56 offen offset:0 sc1\n" \
  "buffer_load_dwordx4 v[20:23], v160, %[rs], s56 offen offset:1024 sc1\n" \
  "buffer_load_dwordx4 v[24:27], v160, %[rs], s56 offen offset:2048 sc1\n" \
  "buffer_load_dwordx4 v[28:31], v160, %[rs], s56 offen offset:3072 sc1\n s_add_u32 s56, s56, 4096\n" \
  "buffer_load_dwordx4 v[32:35], v160, %[rs], s56 offen offset:0 sc1\n" \
  "buffer_load_dwordx4 v[36:39], v160, %[rs], s56 offen offset:1024 sc1\n" \
  "buffer_load_dwordx4 v[40:43], v160, %[rs], s56 offen offset:2048 sc1\n" \
  "buffer_load_dwordx4 v[44:47], v160, %[rs], s56 offen offset:3072 sc1\n s_add_u32 s56, s56, 4096\n" \
  "buffer_load_dwordx4 v[48:51], v160, %[rs], s56 offen offset:0 sc1\n" \
  "buffer_load_dwordx4 v[52:55], v160, %[rs], s56 offen offset:1024 sc1\n" \
  "buffer_load_dwordx4 v[56:59], v160, %[rs], s56 offen offset:2048 sc1\n" \
  "buffer_load_dwordx4 v[60:63], v160, %[rs], s56 offen offset:3072 sc1\n" \
  "s_mov_b32 s55, 0\n" \
  "s_branch 155f\n" \
  "154:\n" \
  "s_sleep 2\n" \
  "s_add_u32 s55, s55, 0x800\n" \
  "s_cbranch_scc1 156f\n" \
  "buffer_load_dwordx2 v[144:145], v151, %[rs], s45 offen sc1\n" \
  "s_waitcnt vmcnt(0)\n" \
  "155:\n" \
  "v_cmp_ne_u64 vcc, s[50:51], v[144:145]\n" \
  "s_cbranch_vccnz 154b\n" \
  "156:\n" \
  "s_add_u32 s54, s46, 0x40000\n" \
  "buffer_load_dwordx4 v[96:99], v108, %[rs], s46 offen sc1\n" \
  "buffer_load_dwordx4 v[100:103], v108, %[rs], s54 offen sc1\n" \
  SST \
  "s_mov_b32 s55, 0\n" \
  "s_branch 158f\n" \
  "157:\n" \
  "s_sleep 5\n" \
  "s_add_u32 s55, s55, 0x400\n" \
  "s_cbranch_scc1 159f\n" \
  "158:\n" \
  "ds_read_b32 v112, v156 offset:104\n" \
  "s_waitcnt lgkmcnt(0)\n" \
  "v_readfirstlane_b32 s54, v112\n" \
  "s_cmp_lt_u32 s54, 16\n" \
  "s_cbranch_scc1 157b\n" \
  "159:\n" \
  SST \
  "ds_read2st64_b64 v[120:123], v152 offset0:64 offset1:68\n" \
  "ds_read2st64_b64 v[124:127], v152 offset0:72 offset1:76\n" \
  "ds_read2st64_b64 v[128:131], v152 offset0:80 offset1:84\n" \
  "ds_read2st64_b64 v[132:135], v152 offset0:88 offset1:92\n" \
  "ds_read_b128 v[136:139], v153 offset:4096\n" \
  "ds_read_b128 v[140:143], v153 offset:4112\n" \
  "s_waitcnt lgkmcnt(4)\n" \
  "v_add_f32 v120, v120, v122\n" \
  "v_add_f32 v121, v121, v123\n" \
  "v_add_f32 v124, v124, v126\n" \
  "v_add_f32 v125, v125, v127\n" \
  "s_waitcnt lgkmcnt(2)\n" \
  "v_add_f32 v128, v128, v130\n" \
  "v_add_f32 v129, v129, v131\n" \
  "v_add_f32 v132, v132, v134\n" \
  "v_add_f32 v133, v133, v135\n" \
  "v_add_f32 v120, v120, v124\n" \
  "v_add_f32 v121, v121, v125\n" \
  "s_waitcnt lgkmcnt(0)\n" \
  "v_add_f32 v136, v136, v138\n" \
  "v_add_f32 v137, v137, v139\n" \
  "v_add_f32 v140, v140, v142\n" \
  "v_add_f32 v141, v141, v143\n" \
  "v_add_f32 v136, v136, v140\n" \
  "v_add_f32 v137, v137, v141\n" \
  "v_add_f32 v136, v136, v137\n" \
  "v_rcp_f32 v137, v136\n" \
  "v_add_f32 v128, v128, v132\n" \
  "v_add_f32 v129, v129, v133\n" \
  "v_add_f32 v120, v120, v128\n" \
  "v_add_f32 v121, v121, v129\n" \
  "v_mul_f32 v120, v120, v137\n" \
  "v_mul_f32 v121, v121, v137\n" \
  "v_cvt_pk_bf16_f32 v122, v120, v121\n" \
  "ds_write_b32 v154, v122 offset:0\n" \
  "s_mov_b64 exec, 1\n" \
  "ds_add_u32 v156, v157 offset:0\n" \
  "s_mov_b64 exec, -1\n" \
  "v_max3_f32 v166, v166, v136, v137\n" \
  "s_mov_b32 s55, 0\n" \
  "s_branch 161f\n" \
  "160:\n" \
  "s_sleep 1\n" \
  "s_add_u32 s55, s55, 0x400\n" \
  "s_cbranch_scc1 162f\n" \
  "161:\n" \
  "ds_read_b32 v112, v156 offset:0\n" \
  "s_waitcnt lgkmcnt(0)\n" \
  "v_readfirstlane_b32 s54, v112\n" \
  "s_cmp_lt_u32 s54, 32\n" \
  "s_cbranch_scc1 160b\n" \
  "162:\n" \
  "ds_read_b128 v[112:115], v155 offset:0\n" \
  "ds_read_b128 v[116:119], v155 offset:64\n" \
  "s_waitcnt vmcnt(0) lgkmcnt(0)\n" \
  "v_smfmac_f32_16x16x64_bf16 v[64:67], v[112:115], v[0:7], v161\n" \
  "v_smfmac_f32_16x16x64_bf16 v[68:71], v[112:115], v[8:15], v161\n" \
  "v_smfmac_f32_16x16x64_bf16 v[72:75], v[112:115], v[16:23], v161\n" \
  "v_smfmac_f32_16x16x64_bf16 v[76:79], v[112:115], v[24:31], v161\n" \
  "v_smfmac_f32_16x16x64_bf16 v[64:67], v[116:119], v[32:39], v161\n" \
  "v_smfmac_f32_16x16x64_bf16 v[68:71], v[116:119], v[40:47], v161\n" \
  "v_smfmac_f32_16x16x64_bf16 v[72:75], v[116:119], v[48:55], v161\n" \
  "v_smfmac_f32_16x16x64_bf16 v[76:79], v[116:119], v[56:63], v161\n" \
  "s_add_u32 s56, s39, s40\n" \
  "s_add_u32 s40, s40, 0x10000\n" \
  "s_and_b32 s40, s40, 0x70000\n" \
  "ds_write_b128 v109, v[96:99]\n" \
  "ds_write_b128 v109, v[100:103] offset:1152\n" \
  "buffer_load_dwordx4 v[0:3], v160, %[rs], s56 offen offset:0 sc1\n" \
  "ds_read2_b32 v[96:97], v110 offset0:0 offset1:4\n" \
  "ds_read2_b32 v[98:99], v110 offset0:8 offset1:12\n" \
  "buffer_load_dwordx4 v[4:7], v160, %[rs], s56 offen offset:1024 sc1\n" \
  "ds_read2_b32 v[100:101], v110 offset0:16 offset1:20\n" \
  "ds_read2_b32 v[102:103], v110 offset0:24 offset1:28\n" \
  "buffer_load_dwordx4 v[8:11], v160, %[rs], s56 offen offset:2048 sc1\n" \
  "s_waitcnt lgkmcnt(0)\n" \
  "v_lshlrev_b32 v112, 16, v96\n" \
  "v_and_b32 v113, 0xffff0000, v96\n" \
  "buffer_load_dwordx4 v[12:15], v160, %[rs], s56 offen offset:3072 sc1\n s_add_u32 s56, s56, 4096\n" \
  "v_mul_f32 v80, v106, v112\n" \
  "v_mul_f32 v88, v107, v113\n" \
  "buffer_load_dwordx4 v[16:19], v160, %[rs], s56 offen offset:0 sc1\n" \
  "v_lshlrev_b32 v112, 16, v97\n" \
  "v_and_b32 v113, 0xffff0000, v97\n" \
  "buffer_load_dwordx4 v[20:23], v160, %[rs], s56 offen offset:1024 sc1\n" \
  "v_mul_f32 v81, v106, v112\n" \
  "v_mul_f32 v89, v107, v113\n" \
  "buffer_load_dwordx4 v[24:27], v160, %[rs], s56 offen offset:2048 sc1\n" \
  "v_lshlrev_b32 v112, 16, v98\n" \
  "v_and_b32 v113, 0xffff0000, v98\n" \
  "buffer_load_dwordx4 v[28:31], v160, %[rs], s56 offen offset:3072 sc1\n s_add_u32 s56, s56, 4096\n" \
  "v_mul_f32 v82, v106, v112\n" \
  "v_mul_f32 v90, v107, v113\n" \
  "buffer_load_dwordx4 v[32:35], v160, %[rs], s56 offen offset:0 sc1\n" \
  "v_lshlrev_b32 v112, 16, v99\n" \
  "v_and_b32 v113, 0xffff0000, v99\n" \
  "buffer_load_dwordx4 v[36:39], v160, %[rs], s56 offen offset:1024 sc1\n" \
  "v_mul_f32 v83, v106, v112\n" \
  "v_mul_f32 v91, v107, v113\n" \
  "buffer_load_dwordx4 v[40:43], v160, %[rs], s56 offen offset:2048 sc1\n" \
  "v_lshlrev_b32 v112, 16, v100\n" \
  "v_and_b32 v113, 0xffff0000, v100\n" \
  "buffer_load_dwordx4 v[44:47], v160, %[rs], s56 offen offset:3072 sc1\n s_add_u32 s56, s56, 4096\n" \
  "v_mul_f32 v84, v106, v112\n" \
  "v_mul_f32 v92, v107, v113\n" \
  "buffer_load_dwordx4 v[48:51], v160, %[rs], s56 offen offset:0 sc1\n" \
  "v_lshlrev_b32 v112, 16, v101\n" \
  "v_and_b32 v113, 0xffff0000, v101\n" \
  "buffer_load_dwordx4 v[52:55], v160, %[rs], s56 offen offset:1024 sc1\n" \
  "v_mul_f32 v85, v106, v112\n" \
  "v_mul_f32 v93, v107, v113\n" \
  "buffer_load_dwordx4 v[56:59], v160, %[rs], s56 offen offset:2048 sc1\n" \
  "v_lshlrev_b32 v112, 16, v102\n" \
  "v_and_b32 v113, 0xffff0000, v102\n" \
  "buffer_load_dwordx4 v[60:63], v160, %[rs], s56 offen offset:3072 sc1\n" \
  "v_mul_f32 v86, v106, v112\n" \
  "v_mul_f32 v94, v107, v113\n" \
  "v_lshlrev_b32 v112, 16, v103\n" \
  "v_and_b32 v113, 0xffff0000, v103\n" \
  "v_mul_f32 v87, v106, v112\n" \
  "v_mul_f32 v95, v107, v113\n" \
  "s_cmp_lg_u32 s47, 0\n" \
  "s_cbranch_scc1 15f\n" \
  "v_mov_b64 v[112:113], s[50:51]\n" \
  "s_mov_b64 exec, 1\n" \
  "buffer_store_dwordx2 v[112:113], off, %[rs], 0 sc1\n" \
  "s_mov_b64 exec, -1\n" \
  "15:\n" \
  SST \
  "s_mov_b32 s55, 0\n" \
  "s_branch 164f\n" \
  "163:\n" \
  "s_sleep 1\n" \
  "s_add_u32 s55, s55, 0x400\n" \
  "s_cbranch_scc1 165f\n" \
  "164:\n" \
  "ds_read_b32 v112, v156 offset:108\n" \
  "s_waitcnt lgkmcnt(0)\n" \
  "v_readfirstlane_b32 s54, v112\n" \
  "s_cmp_lt_u32 s54, 16\n" \
  "s_cbranch_scc1 163b\n" \
  "165:\n" \
  SST \
  "ds_read2st64_b64 v[120:123], v152 offset0:96 offset1:100\n" \
  "ds_read2st64_b64 v[124:127], v152 offset0:104 offset1:108\n" \
  "ds_read2st64_b64 v[128:131], v152 offset0:112 offset1:116\n" \
  "ds_read2st64_b64 v[132:135], v152 offset0:120 offset1:124\n" \
  "ds_read_b128 v[136:139], v153 offset:6144\n" \
  "ds_read_b128 v[140:143], v153 offset:6160\n" \
  "s_waitcnt lgkmcnt(4)\n" \
  "v_add_f32 v120, v120, v122\n" \
  "v_add_f32 v121, v121, v123\n" \
  "v_add_f32 v124, v124, v126\n" \
  "v_add_f32 v125, v125, v127\n" \
  "s_waitcnt lgkmcnt(2)\n" \
  "v_add_f32 v128, v128, v130\n" \
  "v_add_f32 v129, v129, v131\n" \
  "v_add_f32 v132, v132, v134\n" \
  "v_add_f32 v133, v133, v135\n" \
  "v_add_f32 v120, v120, v124\n" \
  "v_add_f32 v121, v121, v125\n" \
  "s_waitcnt lgkmcnt(0)\n" \
  "v_add_f32 v136, v136, v138\n" \
  "v_add_f32 v137, v137, v139\n" \
  "v_add_f32 v140, v140, v142\n" \
  "v_add_f32 v141, v141, v143\n" \
  "v_add_f32 v136, v136, v140\n" \
  "v_add_f32 v137, v137, v141\n" \
  "v_add_f32 v136, v136, v137\n" \
  "v_rcp_f32 v137, v136\n" \
  "v_add_f32 v128, v128, v132\n" \
  "v_add_f32 v129, v129, v133\n" \
  "v_add_f32 v120, v120, v128\n" \
  "v_add_f32 v121, v121, v129\n" \
  "v_mul_f32 v120, v120, v137\n" \
  "v_mul_f32 v121, v121, v137\n" \
  "v_cvt_pk_bf16_f32 v122, v120, v121\n" \
  "ds_write_b32 v154, v122 offset:2048\n" \
  "s_mov_b64 exec, 1\n" \
  "ds_add_u32 v156, v157 offset:0\n" \
  "s_mov_b64 exec, -1\n" \
  "v_max3_f32 v166, v166, v136, v137\n" \
  "s_mov_b32 s55, 0\n" \
  "s_branch 167f\n" \
  "166:\n" \
  "s_sleep 1\n" \
  "s_add_u32 s55, s55, 0x400\n" \
  "s_cbranch_scc1 168f\n" \
  "167:\n" \
  "ds_read_b32 v112, v156 offset:0\n" \
  "s_waitcnt lgkmcnt(0)\n" \
  "v_readfirstlane_b32 s54, v112\n" \
  "s_cmp_lt_u32 s54, 36\n" \
  "s_cbranch_scc1 166b\n" \
  "168:\n" \
  "ds_read_b128 v[112:115], v155 offset:2048\n" \
  "ds_read_b128 v[116:119], v155 offset:2112\n" \
  "s_waitcnt vmcnt(0) lgkmcnt(0)\n" \
  "v_smfmac_f32_16x16x64_bf16 v[64:67], v[112:115], v[0:7], v161\n" \
  "v_smfmac_f32_16x16x64_bf16 v[68:71], v[112:115], v[8:15], v161\n" \
  "v_smfmac_f32_16x16x64_bf16 v[72:75], v[112:115], v[16:23], v161\n" \
  "v_smfmac_f32_16x16x64_bf16 v[76:79], v[112:115], v[24:31], v161\n" \
  "v_smfmac_f32_16x16x64_bf16 v[64:67], v[116:119], v[32:39], v161\n" \
  "v_smfmac_f32_16x16x64_bf16 v[68:71], v[116:119], v[40:47], v161\n" \
  "v_smfmac_f32_16x16x64_bf16 v[72:75], v[116:119], v[48:55], v161\n" \
  "v_smfmac_f32_16x16x64_bf16 v[76:79], v[116:119], v[56:63], v161\n" \
  "s_nop 7\n" \
  "v_pk_fma_f32 v[8:9], v[64:65], v[104:105], v[80:81] op_sel_hi:[1,0,1]\n" \
  "v_pk_fma_f32 v[10:11], v[66:67], v[104:105], v[88:89] op_sel:[0,1,0]\n" \
  "v_pk_fma_f32 v[12:13], v[68:69], v[104:105], v[82:83] op_sel_hi:[1,0,1]\n" \
  "v_pk_fma_f32 v[14:15], v[70:71], v[104:105], v[90:91] op_sel:[0,1,0]\n" \
  "v_pk_fma_f32 v[16:17], v[72:73], v[104:105], v[84:85] op_sel_hi:[1,0,1]\n" \
  "v_pk_fma_f32 v[18:19], v[74:75], v[104:105], v[92:93] op_sel:[0,1,0]\n" \
  "v_pk_fma_f32 v[20:21], v[76:77], v[104:105], v[86:87] op_sel_hi:[1,0,1]\n" \
  "v_pk_fma_f32 v[22:23], v[78:79], v[104:105], v[94:95] op_sel:[0,1,0]\n" \
  "v_cvt_pk_bf16_f32 v0, v8, v9\n" \
  "v_cvt_pk_bf16_f32 v1, v12, v13\n" \
  "v_cvt_pk_bf16_f32 v2, v16, v17\n" \
  "v_cvt_pk_bf16_f32 v3, v20, v21\n" \
  "buffer_store_dwordx4 v[0:3], v111, %[ro], s48 offen sc1\n" \
  "v_cvt_pk_bf16_f32 v4, v10, v11\n" \
  "v_cvt_pk_bf16_f32 v5, v14, v15\n" \
  "v_cvt_pk_bf16_f32 v6, v18, v19\n" \
  "v_cvt_pk_bf16_f32 v7, v22, v23\n" \
  "buffer_store_dwordx4 v[4:7], v111, %[ro], s48 offen offset:1024 sc1\n" \
  "s_nop 1\n"
#define SVC_REDO \
  "v_mov_b64 v[112:113], 0\n" \
  "v_mov_b64 v[114:115], 0\n" \
  "s_mov_b32 s57, 0\n" \
  "s_mov_b32 s58, 40\n" \
  "s_nop 0\n" \
  "v_mfma_f32_32x32x16_bf16 v[64:79], v[112:115], v[112:115], 0\n" \
  "21:\n" \
  "s_add_u32 s56, s39, s40\n" \
  "s_add_u32 s40, s40, 0x10000\n" \
  "s_and_b32 s40, s40, 0x70000\n" \
  "buffer_load_dwordx4 v[0:3], v160, %[rs], s56 offen offset:0 sc1\n" \
  "buffer_load_dwordx4 v[4:7], v160, %[rs], s56 offen offset:1024 sc1\n" \
  "buffer_load_dwordx4 v[8:11], v160, %[rs], s56 offen offset:2048 sc1\n" \
  "buffer_load_dwordx4 v[12:15], v160, %[rs], s56 offen offset:3072 sc1\n s_add_u32 s56, s56, 4096\n" \
  "buffer_load_dwordx4 v[16:19], v160, %[rs], s56 offen offset:0 sc1\n" \
  "buffer_load_dwordx4 v[20:23], v160, %[rs], s56 offen offset:1024 sc1\n" \
  "buffer_load_dwordx4 v[24:27], v160, %[rs], s56 offen offset:2048 sc1\n" \
  "buffer_load_dwordx4 v[28:31], v160, %[rs], s56 offen offset:3072 sc1\n s_add_u32 s56, s56, 4096\n" \
  "buffer_load_dwordx4 v[32:35], v160, %[rs], s56 offen offset:0 sc1\n" \
  "buffer_load_dwordx4 v[36:39], v160, %[rs], s56 offen offset:1024 sc1\n" \
  "buffer_load_dwordx4 v[40:43], v160, %[rs], s56 offen offset:2048 sc1\n" \
  "buffer_load_dwordx4 v[44:47], v160, %[rs], s56 offen offset:3072 sc1\n s_add_u32 s56, s56, 4096\n" \
  "buffer_load_dwordx4 v[48:51], v160, %[rs], s56 offen offset:0 sc1\n" \
  "buffer_load_dwordx4 v[52:55], v160, %[rs], s56 offen offset:1024 sc1\n" \
  "buffer_load_dwordx4 v[56:59], v160, %[rs], s56 offen offset:2048 sc1\n" \
  "buffer_load_dwordx4 v[60:63], v160, %[rs], s56 offen offset:3072 sc1\n" \
  "s_barrier\n" \
  "s_barrier\n" \
  "s_lshl_b32 s54, s57, 2\n" \
  "v_add_u32 v113, s54, v156\n" \
  "s_lshl_b32 s54, s57, 14\n" \
  "v_add_u32 v114, s54, v152\n" \
  "s_lshl_b32 s54, s57, 11\n" \
  "v_add_u32 v115, s54, v153\n" \
  "ds_read2st64_b64 v[120:123], v114 offset0:0 offset1:4\n" \
  "ds_read2st64_b64 v[124:127], v114 offset0:8 offset1:12\n" \
  "ds_read2st64_b64 v[128:131], v114 offset0:16 offset1:20\n" \
  "ds_read2st64_b64 v[132:135], v114 offset0:24 offset1:28\n" \
  "ds_read_b128 v[136:139], v115 offset:0\n" \
  "ds_read_b128 v[140:143], v115 offset:16\n" \
  "s_waitcnt lgkmcnt(4)\n" \
  "v_add_f32 v120, v120, v122\n" \
  "v_add_f32 v121, v121, v123\n" \
  "v_add_f32 v124, v124, v126\n" \
  "v_add_f32 v125, v125, v127\n" \
  "s_waitcnt lgkmcnt(2)\n" \
  "v_add_f32 v128, v128, v130\n" \
  "v_add_f32 v129, v129, v131\n" \
  "v_add_f32 v132, v132, v134\n" \
  "v_add_f32 v133, v133, v135\n" \
  "v_add_f32 v120, v120, v124\n" \
  "v_add_f32 v121, v121, v125\n" \
  "s_waitcnt lgkmcnt(0)\n" \
  "v_add_f32 v136, v136, v138\n" \
  "v_add_f32 v137, v137, v139\n" \
  "v_add_f32 v140, v140, v142\n" \
  "v_add_f32 v141, v141, v143\n" \
  "v_add_f32 v136, v136, v140\n" \
  "v_add_f32 v137, v137, v141\n" \
  "v_add_f32 v136, v136, v137\n" \
  "v_rcp_f32 v137, v136\n" \
  "v_add_f32 v128, v128, v132\n" \
  "v_add_f32 v129, v129, v133\n" \
  "v_add_f32 v120, v120, v128\n" \
  "v_add_f32 v121, v121, v129\n" \
  "v_mul_f32 v120, v120, v137\n" \
  "v_mul_f32 v121, v121, v137\n" \
  "v_cvt_pk_bf16_f32 v122, v120, v121\n" \
  "ds_write_b32 v154, v122 offset:0\n" \
  "s_mov_b64 exec, 1\n" \
  "ds_add_u32 v156, v157 offset:0\n" \
  "s_mov_b64 exec, -1\n" \
  "v_max3_f32 v166, v166, v136, v137\n" \
  "v_xor_b32 v154, 0x800, v154\n" \
  "s_mov_b32 s55, 0\n" \
  "s_branch 202f\n" \
  "201:\n" \
  "s_sleep 1\n" \
  "s_add_u32 s55, s55, 0x400\n" \
  "s_cbranch_scc1 203f\n" \
  "202:\n" \
  "ds_read_b32 v112, v156 offset:0\n" \
  "s_waitcnt lgkmcnt(0)\n" \
  "v_readfirstlane_b32 s54, v112\n" \
  "s_cmp_lt_u32 s54, s58\n" \
  "s_cbranch_scc1 201b\n" \
  "203:\n" \
  "ds_read_b128 v[112:115], v155 offset:0\n" \
  "ds_read_b128 v[116:119], v155 offset:64\n" \
  "v_xor_b32 v155, 0x800, v155\n" \
  "s_waitcnt vmcnt(0) lgkmcnt(0)\n" \
  "v_smfmac_f32_16x16x64_bf16 v[64:67], v[112:115], v[0:7], v161\n" \
  "v_smfmac_f32_16x16x64_bf16 v[68:71], v[112:115], v[8:15], v161\n" \
  "v_smfmac_f32_16x16x64_bf16 v[72:75], v[112:115], v[16:23], v161\n" \
  "v_smfmac_f32_16x16x64_bf16 v[76:79], v[112:115], v[24:31], v161\n" \
  "v_smfmac_f32_16x16x64_bf16 v[64:67], v[116:119], v[32:39], v161\n" \
  "v_smfmac_f32_16x16x64_bf16 v[68:71], v[116:119], v[40:47], v161\n" \
  "v_smfmac_f32_16x16x64_bf16 v[72:75], v[116:119], v[48:55], v161\n" \
  "v_smfmac_f32_16x16x64_bf16 v[76:79], v[116:119], v[56:63], v161\n" \
  "s_xor_b32 s57, s57, 1\n" \
  "s_add_u32 s58, s58, 4\n" \
  "s_cmp_lg_u32 s58, 72\n" \
  "s_cbranch_scc1 21b\n" \
  "s_nop 7\n" \
  "v_pk_fma_f32 v[8:9], v[64:65], v[104:105], v[80:81] op_sel_hi:[1,0,1]\n" \
  "v_pk_fma_f32 v[10:11], v[66:67], v[104:105], v[88:89] op_sel:[0,1,0]\n" \
  "v_pk_fma_f32 v[12:13], v[68:69], v[104:105], v[82:83] op_sel_hi:[1,0,1]\n" \
  "v_pk_fma_f32 v[14:15], v[70:71], v[104:105], v[90:91] op_sel:[0,1,0]\n" \
  "v_pk_fma_f32 v[16:17], v[72:73], v[104:105], v[84:85] op_sel_hi:[1,0,1]\n" \
  "v_pk_fma_f32 v[18:19], v[74:75], v[104:105], v[92:93] op_sel:[0,1,0]\n" \
  "v_pk_fma_f32 v[20:21], v[76:77], v[104:105], v[86:87] op_sel_hi:[1,0,1]\n" \
  "v_pk_fma_f32 v[22:23], v[78:79], v[104:105], v[94:95] op_sel:[0,1,0]\n" \
  "v_cvt_pk_bf16_f32 v0, v8, v9\n" \
  "v_cvt_pk_bf16_f32 v1, v12, v13\n" \
  "v_cvt_pk_bf16_f32 v2, v16, v17\n" \
  "v_cvt_pk_bf16_f32 v3, v20, v21\n" \
  "buffer_store_dwordx4 v[0:3], v111, %[ro], s48 offen sc1\n" \
  "v_cvt_pk_bf16_f32 v4, v10, v11\n" \
  "v_cvt_pk_bf16_f32 v5, v14, v15\n" \
  "v_cvt_pk_bf16_f32 v6, v18, v19\n" \
  "v_cvt_pk_bf16_f32 v7, v22, v23\n" \
  "buffer_store_dwordx4 v[4:7], v111, %[ro], s48 offen offset:1024 sc1\n" \
  "s_nop 1\n"
static_assert(PW == 2048 && XROW == 528 && RCOPY == 4160 && C_SVC == 0 && C_KV == 8 && C_F2 == 12 && C_Q == 32 && C_XY == 48 && C_KG == 56 && C_P == 96 && CH_YS - CH_U == 4608 && YSW == 4096,
              "offsets are spelled out in the asm");
typedef float f32x32 __attribute__((ext_vector_type(32)));

DI void set_word(const Params& p, u32 off, u64 tag) { __hip_atomic_store((GAS u64*)(p.ws + off), tag, RLX_AGENT); }
DI void reset_issue(rsrc_t rs, u32x4& v, u32 voff, u32 soff, u64 mask) {
  asm volatile("s_mov_b64 exec, %4\n\tbuffer_atomic_cmpswap_x2 %0, %1, %2, %3 offen sc0\n\ts_mov_b64 exec, -1\n\ts_nop 1"
               : "+v"(v) : "v"(voff), "s"(rs), "s"(soff), "s"(mask) : "memory");
}
DI u32 arrive_issue(rsrc_t rs, int kind, int m, u32 on) {
  u32 v = 1u;
  asm volatile("s_nop 0\n\ts_mov_b32 exec_lo, %3\n\ts_mov_b32 exec_hi, 0\n\tbuffer_atomic_add %0, off, %1, %2 sc0\n\ts_mov_b64 exec, -1"
               : "+v"(v) : "s"(rs), "s"(WS_CNT + ((m >> 5) * 3 + kind) * 256), "s"(on) : "memory");
  return v;
}
DI void signal_go(const Params& p, int kind, int m, u32 lane, u32 old, u64 tag) {
  asm volatile("s_waitcnt vmcnt(0)" : "+v"(old) :: "memory");
  if (__builtin_amdgcn_readfirstlane(old) == 31u && lane < 8) set_word(p, WS_GO + kind * 8192 + lane * 1024 + (m >> 5) * 128, tag);
}

DI void svc_role(lptr lds, u32 m, u32 sw, u32 lane, u64 pc) {
  __builtin_amdgcn_s_setprio(3);
#define PADTO(t) do {} while (0)
  STAMP_INIT(8 + sw);
  STAMP(0);
  const u32 i = m >> 3, j = m & 7, T0 = 64 * i;
  const u32 l16 = lane & 15, g = lane >> 4, q4 = l16 >> 2, pp = l16 & 3;
  const u32 cl = sw >> 1, ch = 2 * m + cl, n = 64 * (sw & 1) + lane;
  u32 cwarm = 0;
  {
    if (sw == 1 && lane < 6) cwarm = gld<u32>((const char*)(pc & ~(u64)127) + (m & 31) * 704 + lane * 128);
  }
  u32 po = (ch * NMODE + n) * 2, po2 = po ^ 128;
  asm volatile("" : "+v"(po), "+v"(po2));
  const Params p = wait_params();
  const rsrc_t rs = ws_rsrc(p.ws);
  const u32 lo16 = lane * 16;
  const u32x2 ep = bld2(rs, 0, WS_EP);
  const u32 cvo = WS_CNT + (m >> 5) * 768 + (lane < 3 ? lane : 0) * 256;
  u32x2 cseen = {0u, 0u};
  if (sw == 0) cseen = bld2(rs, cvo, 0);
  const u16 r_lar = gld<u16>((const char*)p.logA + po), r_aim = gld<u16>((const char*)p.Aim + po);
  const u16 r_cr = gld<u16>((const char*)p.Cre + po), r_ci = gld<u16>((const char*)p.Cim + po);
  const u16 r_cr2 = gld<u16>((const char*)p.Cre + po2), r_ci2 = gld<u16>((const char*)p.Cim + po2);
  const u16 r_ldt = gld<u16>(p.logdt + ch), r_dsk = gld<u16>(p.Dskip + ch);
  __builtin_amdgcn_sched_barrier(0);
  const u16* xsrc = p.x + (size_t)(8 * m + 2 * g) * DM + 128 * sw + 8 * l16;
  const u32x4 gx0 = gld<u32x4>(xsrc), gx1 = gld<u32x4>(xsrc + DM), gw = gld<u32x4>(p.gatew + 128 * sw + 8 * l16);
  const int wu = m >> 1, whf = wu & 1, wkb = (wu >> 1) & 1, ws2 = (wu >> 2) & 3, wr = wu >> 4;
  const bool wo_lane = (sw == 3) && ((lane >> 5) == (m & 1));
  u32x4 wv4[4];
  if (wo_lane) {
    const u16* src = p.wo + (size_t)(64 * wr + 32 * wkb + 16 * whf + 4 * g) * DM + 128 * ws2 + 8 * l16;
#pragma unroll
    for (int e = 0; e < 4; ++e) wv4[e] = gld<u32x4>(src + e * DM);
  }
  __builtin_amdgcn_sched_barrier(0);
  u32x16 av, bv;
  {
    const u32 lb = (u32)(uintptr_t)lds, ib = lb + L_XY + cl * XYCH, tt = lane & 7, dp = lane >> 3, hf = sw & 1, rho = 15 - l16;
    av[0] = lb + L_PART + (dp >> 1) * 512 + (8 * sw + tt) * 16 + 8 * (dp & 1);
    av[1] = lb + L_L + (8 * sw + tt) * 64;
    av[2] = lb + L_O + tt * OROW + 32 * sw + 4 * dp;
    av[3] = lb + L_O + (l16 >> 1) * OROW + 16 * g;
    av[4] = lb + L_CTL;
    av[5] = 1u;
    av[6] = ib + ((3 - l16) & 3) * RCOPY + 472 - 256 * hf + 16 * g - 8 * (l16 >> 2);
    av[7] = ib + CH_U + 480 - 256 * hf + 32 * l16 + 16 * g;
    av[8] = lo16;
    av[9] = (lane & 1) ? 0xDDDDu : 0x8888u;
    av[10] = ib + CH_U + 512 + hf * 2048 + lo16;
    av[11] = lb + L_CTL + 4 * cl;
    av[12] = 0u; av[13] = 0u; av[14] = 0u; av[15] = 0u;
    bv[0] = ib + n * 4;
    bv[1] = ib + (47 + l16) * XROW + 16 * g;
    const u32 r1 = (rho + ((rho >> 1) & 1)) >> 1, r2 = (rho + 1 + ((rho >> 2) & 1)) >> 2, r3 = (rho + 3 + ((rho >> 3) & 1)) >> 3;
    bv[2] = ib + 16 * g + (hf ? 35 + r3 : rho) * XROW;
    bv[3] = ib + 16 * g + (hf ? 38 + r3 : 16 + r1) * XROW;
    bv[4] = ib + 16 * g + (hf ? 41 + r3 : 25 + r2) * XROW;
    bv[5] = ib + 16 * g + (hf ? 44 + r3 : 30 + r2) * XROW;
    bv[6] = lb + L_QR + sw * 2048 + lo16;
    bv[7] = 0x7f000000u;
    bv[8] = 0u;
    bv[9] = ib + (2032 - 1024 * hf - 768 - 16 * l16 + 4 * g) * 2;
    bv[10] = ib + (hf ? CH_U + lane * 8 : ((lane >> 2) & 3) * RCOPY + 4096 + (lane & 3) * 8);
    bv[11] = ib + (!hf && (lane & 3) < ((lane >> 2) & 3) ? ((lane >> 2) & 3) * RCOPY + 4094 - 2 * (lane & 3) : 4100);
    bv[12] = 0u;
    bv[13] = (lane & 7) * 128;
    bv[14] = 0u; bv[15] = 0u;
    if (sw == 2) {
      lds_st<u32x4>(lds + L_TAB + 1024 + lo16, u32x4{lane * (SEQ * 2) + 16 * m, (lane >> 3) * 144 + (lane & 7) * 16, l16 * 144 + 4 * g, g * (4 * DM) + l16 * 16});
      lds_st<u32x2>(lds + L_TAB + 2048 + lo16, u32x2{32 * l16 + 8 * g, lo16 & 128});
    }
    asm volatile("" : "+v"(av), "+v"(bv));
  }
  const u32 taglo = __builtin_amdgcn_readfirstlane(ep[0]) * (u32)TAG_A0 + (u32)TAG_C0;
  const u32 taghi = __builtin_amdgcn_readfirstlane(ep[1]) * (u32)TAG_A1 + (u32)TAG_C1;
  const u64 tag = (u64)taglo | ((u64)taghi << 32);
  u32x4 cas = {0u, 0u, 0u, 0u};
  u64 cmask = 0;
  if (sw == 0) {
    cas = u32x4{0u, (u32)__builtin_amdgcn_readfirstlane(ep[0]) * (u32)TAG_A0 + (u32)TAG_C0, cseen[0], cseen[1]};
    cmask = __ballot(lane < 3 && cas[3] != cas[1]);
  }
  reset_issue(rs, cas, cvo, 0u, cmask);
    if (sw == 0) {
      const u32 tb = T0 + 16 * (j >> 1);
      u32 off = (j & 1) ? WS_VF + (tb >> 4) * 512 + lane * 8 : WS_KF + (tb >> 5) * 1024 + (tb & 31) * 16 + (lane >> 5) * 512 + (lane & 31) * 8;
      u32 pa = (u32)(uintptr_t)lds + L_KVP + (((lane >> 1) & 15) + 16 * (2 * (lane >> 5) + (lane & 1))) * 16;
      float ks = (j & 1) ? 1.f : 0.25f * 1.4426950408889634f;
      asm volatile("" : "+v"(off), "+v"(pa), "+v"(ks));
      PROBE(40);
      cnt_wait<false>(lds, C_KV, 8u);
      PROBE(41);
      f32x4 a[8];
#pragma unroll
      for (int w = 0; w < 8; ++w) a[w] = lds_ld<f32x4>((lptr)(uintptr_t)pa + w * 1024);
      cnt_add(lds, C_KV);
      const f32x4 t = (((a[0] + a[1]) + (a[2] + a[3])) + ((a[4] + a[5]) + (a[6] + a[7]))) * ks;
      bst2(rs, off, 0, u32x2{pkbf(t[0], t[1]), pkbf(t[2], t[3])});
      STAMP(2);
    }
  if (sw == 1) STAMP(1);
  u32x32 ga, gb;
  u32x16 xt = {};
  {
    const float cr = bf2f(r_cr), ci = bf2f(r_ci);
    const float dt = bfr(expf(bf2f(r_ldt)));
    const float zr = -bfr(expf(bf2f(r_lar))) * dt, zi = bf2f(r_aim) * dt;
    const float lr = zr * 1.4426950408889634f, fr = zi * 0.15915494309189535f;
    float s = wave_max(fmaxf(fmaxf(fabsf(cr), fabsf(ci)), fmaxf(fabsf(bf2f(r_cr2)), fabsf(bf2f(r_ci2)))));
    s = fmaxf(s, 1e-30f);
    const float inv = 16384.f * __builtin_amdgcn_rcpf(s);
    f32x2 c0 = {cr * inv, -ci * inv};
    f32x2 e1 = cexp_t(lr, -fr, 1.f), e16 = cexp_t(lr, fr, 16.f), e256 = cexp_t(lr, -fr, 256.f), y8 = cexp_t(lr, fr, 128.f) * 16384.f;
    f32x2 e2 = cmul(e1, e1), e4 = cmul(e2, e2), e8 = cmul(e4, e4);
    asm volatile("" : "+v"(c0), "+v"(e1), "+v"(e16), "+v"(e256), "+v"(y8), "+v"(e2), "+v"(e4), "+v"(e8));
    if (sw == 0) {
#pragma unroll 1
      for (u32 spins = 0; spins < 1024u; ++spins) {
        asm volatile("s_waitcnt vmcnt(0)" : "+v"(cas) :: "memory");
        const u64 again = __ballot(lane < 3 && cas[1] != taglo && (cas[0] != cas[2] || cas[1] != cas[3]));
        if (again == 0ull) break;
        cas = u32x4{0u, taglo, cas[0], cas[1]};
        reset_issue(rs, cas, cvo, 0u, again);
      }
      drain_vm();
    }
    const u32 arr = arrive_issue(rs, 0, m, 1u >> sw);
    if (sw == 0) { cnt_add(lds, C_KV); STAMP(29); }
    STAMP(4);
    u32 gv[63];
    f32x2 st[8];
    st[0] = c0;
    {
      f32x2 ea = c0, eb = cmul(c0, e8), ya = {16384.f, 0.f}, yb = y8;
#pragma unroll
      for (int r = 0; r < 8; ++r) {
        gv[r] = pkh(ea); gv[8 + r] = pkh(eb); gv[47 + r] = pkh(ya); gv[55 + r] = pkh(yb);
        if (r < 7) { ea = cmul(ea, e1); eb = cmul(eb, e1); ya = cmul(ya, e16); yb = cmul(yb, e16); st[r + 1] = cmul(st[r], e256); }
        asm volatile("" : "+v"(gv[r]), "+v"(gv[8 + r]), "+v"(gv[47 + r]), "+v"(gv[55 + r]));
        if (r == 7 && sw == 0) { signal_go(p, 0, m, lane, arr, tag); STAMP(3); }
        __builtin_amdgcn_sched_barrier(0);
      }
      f32x2 a = st[1], b = cmul(st[1], e8), c = st[2], d = st[3];
#pragma unroll
      for (int q = 0; q < 5; ++q) {
        if (q < 4) { gv[16 + q] = pkh(a); asm volatile("" : "+v"(gv[16 + q])); }
        gv[20 + q] = pkh(b); gv[25 + q] = pkh(c); gv[30 + q] = pkh(d);
        if (q < 4) { if (q < 3) a = cmul(a, e2); b = cmul(b, e2); c = cmul(c, e4); d = cmul(d, e4); }
        asm volatile("" : "+v"(gv[20 + q]), "+v"(gv[25 + q]), "+v"(gv[30 + q]));
        __builtin_amdgcn_sched_barrier(0);
      }
#pragma unroll
      for (int q = 0; q < 3; ++q) {
#pragma unroll
        for (int T = 4; T < 8; ++T) { gv[35 + 3 * (T - 4) + q] = pkh(st[T]); if (q < 2) st[T] = cmul(st[T], e8); }
        asm volatile("" : "+v"(gv[35 + q]), "+v"(gv[38 + q]), "+v"(gv[41 + q]), "+v"(gv[44 + q]));
        __builtin_amdgcn_sched_barrier(0);
      }
    }
    STAMP(5);
    {
      float s0 = 0.f, s1 = 0.f;
#pragma unroll
      for (int e = 0; e < 4; ++e) {
        const u32 p0 = pkbf(bflo(gx0[e]) * bflo(gw[e]), bfhi(gx0[e]) * bfhi(gw[e]));
        const u32 p1 = pkbf(bflo(gx1[e]) * bflo(gw[e]), bfhi(gx1[e]) * bfhi(gw[e]));
        s0 += bflo(p0) + bfhi(p0);
        s1 += bflo(p1) + bfhi(p1);
      }
      s0 = row_sum(s0); s1 = row_sum(s1);
      if (l16 == 0) { lds_st<float>(lds + L_G + (2 * g) * 16 + sw * 4, s0); lds_st<float>(lds + L_G + (2 * g + 1) * 16 + sw * 4, s1); }
    }
    asm volatile("" :: "v"(cwarm));
    if (wo_lane) {
      const u32 off = WS_WOF + (((wr * 4 + ws2) * 2 + wkb) * 8 + whf) * 1024 + lo16;
#pragma unroll
      for (int nt = 0; nt < 4; ++nt) { u32x4 o = {wv4[0][nt], wv4[1][nt], wv4[2][nt], wv4[3][nt]}; bst4(rs, off + nt * 2048, 0, o); }
    }
    cnt_add(lds, C_XT);
    if (sw == 1 || sw == 2) {
      const u32 row = 8 * g + q4, cu = 4 * (sw - 1) + (pp >> 1);
      const u32 xb = (u32)(uintptr_t)lds + L_XY + j * 8192 + row * 128 + 8 * (pp & 1);
      u32 lo0 = xb + ((cu ^ ((row >> 1) & 7)) << 4), hi0 = xb + 512 + ((cu ^ (((row >> 1) & 7) + 2)) << 4);
      u32 tw = (u32)(uintptr_t)lds + L_KVP + (sw - 1) * 4096 + l16 * 128 + ((g ^ (l16 & 7)) << 4);
      u32 tr = (u32)(uintptr_t)lds + L_KVP + (sw - 1) * 4096 + (lane >> 3) * 128 + (((lane & 7) ^ (lane >> 3)) << 4);
      u32 off = WS_XT + ((64 * j + 32 * (sw - 1) + (lane >> 3)) * SEQ + T0 + 8 * (lane & 7)) * 2;
      asm volatile("" : "+v"(lo0), "+v"(hi0), "+v"(tw), "+v"(tr), "+v"(off));
      cnt_wait(lds, C_KV, 9u);
      cnt_wait(lds, C_WQ + (j & 4), 4u);
#define LP(a) ((lptr)(uintptr_t)(a))
      const bf16x8 v0 = trfrag(LP(lo0), LP(hi0)), v1 = trfrag(LP(lo0) + 4096, LP(hi0) + 4096);
      const bf16x8 v2 = trfrag(LP(lo0 ^ 32), LP(hi0 ^ 32)), v3 = trfrag(LP(lo0 ^ 32) + 4096, LP(hi0 ^ 32) + 4096);
      lds_st<bf16x8>(LP(tw), v0); lds_st<bf16x8>(LP(tw ^ 64), v1);
      lds_st<bf16x8>(LP(tw) + 2048, v2); lds_st<bf16x8>(LP(tw ^ 64) + 2048, v3);
#pragma unroll
      for (int e = 0; e < 4; ++e) {
        const u32x4 t = lds_ld<u32x4>(LP(tr) + e * 1024);
        xt[4 * e] = t[0]; xt[4 * e + 1] = t[1]; xt[4 * e + 2] = t[2]; xt[4 * e + 3] = t[3];
      }
      bv[7] = off;
      cnt_add(lds, C_Q);
    }
    if (sw == 3) {
#pragma unroll 1
      for (u32 spins = 0; spins < (1u << 21); ++spins) {
        const u32x2 f = bld2(rs, 0, WS_GO + (m >> 5) * 1024 + (m & 7) * 128);
        if (__all(f[0] == taglo && f[1] == taghi)) break;
        __builtin_amdgcn_s_sleep(1);
      }
      u32 d = bld1(rs, ((lane >> 4) & 1) * 65536 + (lane & 15) * 128, WS_KF + (m & 7) * 8192 + ((m >> 3) & 3) * 2048);
      asm volatile("" :: "v"(d));
    }
    if (sw == 0) {
      cnt_wait(lds, C_XT, 4u);
      const f32x4 za = lds_ld<f32x4>(lds + L_G + g * 32), zb = lds_ld<f32x4>(lds + L_G + g * 32 + 16);
      const float z0 = bfr((za[0] + za[1]) + (za[2] + za[3])), z1 = bfr((zb[0] + zb[1]) + (zb[2] + zb[3]));
      const float g0 = bfr(__builtin_amdgcn_rcpf(1.f + __expf(-z0))), g1 = bfr(__builtin_amdgcn_rcpf(1.f + __expf(-z1)));
      const float h0 = bfr(1.f - g0), h1 = bfr(1.f - g1);
      lds_st<f32x4>(lds + L_TAB + lo16, f32x4{g0, g1, h0, h1});
    }
    bv[12] = __float_as_uint((2.f * dt) * (1.f / 16384.f) / inv);
#pragma unroll
    for (int r = 0; r < 32; ++r) { ga[r] = gv[r]; gb[r] = gv[r < 31 ? 32 + r : 62]; }
  }
  u32x16 sc;
  sc[0] = WS_QP + (m * 8 + 2 * sw) * 1024;
  sc[1] = WS_GO + 8192 + (m >> 5) * 1024;
  sc[2] = WS_XT + ch * (SEQ * 2) + (sw & 1) * 2048;
  sc[3] = WS_WOF + sw * 16384;
  sc[4] = j * 65536;
  sc[5] = WS_YT + ch * (SEQ * 2) + (sw & 1) * 2048;
  sc[6] = (u32)(uintptr_t)lds + L_XY + cl * XYCH + CH_YS + ((sw & 1) ^ 1) * YSW;
  sc[7] = WS_CNT + (m >> 5) * 768;
  sc[8] = WS_GO + 8192 + (m >> 5) * 128;
  sc[9] = WS_GO + 16384 + (m >> 5) * 1024 + sw * 256;
  sc[10] = WS_YT + 128 * sw * (SEQ * 2);
  sc[11] = m | sw;
  sc[12] = m * (8 * DM * 2) + 256 * sw;
  sc[13] = sw & 1;
  sc[14] = taglo; sc[15] = taghi;
  const rsrc_t ro = __builtin_amdgcn_make_buffer_rsrc(p.out, 0, SEQ * DM * 2, 0x00020000);
  f32x16 oacc = __builtin_bit_cast(f32x16, xt);
  f32x32 epi;
  u32 dtap = (n == 48) ? (u32)r_dsk << 16 : 0u;
  asm volatile(SVC_MAIN
    : "+&{v[0:31]}"(ga), "+&{v[32:63]}"(gb), "+&{v[64:79]}"(oacc), "=&{v[80:111]}"(epi), "+&{v[136:151]}"(bv), "+&{v[152:167]}"(av), "+&{s[36:51]}"(sc), "+&{v135}"(dtap)
    : [rs] "s"(rs), [ro] "s"(ro), [tab] "s"((u32)(uintptr_t)lds + L_TAB),
      [qs0] "s"(WS_QP + ((8 * i + 2 * sw) * 8 + ((j - 2 * sw) & 7)) * 1024), [qs1] "s"(WS_QP + ((8 * i + 2 * sw + 1) * 8 + ((j - 2 * sw - 1) & 7)) * 1024) : "memory", "scc", "vcc", "v112", "v113", "v114", "v115", "v116", "v117", "v118", "v119", CL10(12),
      "v130", "v131", "v132", "v133", "v134", "s54", "s55", "s56", "s57", "s58", "s59", "s60", "s61", "s62", "s63");
  STAMP(28);
  PADTO(PAD_END);
  STAMP_DUMP();
  {
    float z = 0.f;
#pragma unroll
    for (int e = 0; e < 16; ++e) z = __builtin_fmaf(oacc[e], 0.f, z);
    if (!(z == 0.f && __uint_as_float(av[14]) < 7.9228163e28f)) lds_st<u32>(lds + L_CTL + C_BAD, 1u);
  }
  blk_barrier();
  if (__builtin_amdgcn_readfirstlane(lds_ld<u32>(lds + L_CTL + C_BAD)) != 0u) {
    asm volatile(SVC_REDO
      : "+&{v[80:111]}"(epi), "+&{v[152:167]}"(av), "+&{s[36:51]}"(sc)
      : [rs] "s"(rs), [ro] "s"(ro) : "memory", "scc", "vcc", CL10(), CL10(1), CL10(2), CL10(3), CL10(4), CL10(5), CL10(6), CL10(7),
        "v112", "v113", "v114", "v115", "v116", "v117", "v118", "v119", CL10(12), CL10(13), CL10(14), "v150", "v151",
        "s54", "s55", "s56", "s57", "s58", "s59", "s60", "s61", "s62", "s63");
  }
}

__global__ __launch_bounds__(768, 3) void k_fused(Params) {
  __shared__ __attribute__((aligned(1024))) char smem[L_BYTES + STAMP_BYTES];
  lptr lds = (lptr)smem;
  if (threadIdx.x < 32) lds_st<u32>(lds + L_CTL + threadIdx.x * 4, 0u);
  u64 pc;
  asm volatile("s_waitcnt lgkmcnt(0)\n\ts_load_dwordx16 s[72:87], %1, 0\n\ts_load_dwordx8 s[88:95], %1, 0x40\n\ts_load_dwordx4 s[96:99], %1, 0x60\n\t"
               "s_getpc_b64 %0\n\ts_barrier"
               : "=&s"(pc) : "s"(__builtin_amdgcn_kernarg_segment_ptr())
               : "memory", "s72", "s73", "s74", "s75", "s76", "s77", "s78", "s79", "s80", "s81", "s82", "s83", "s84", "s85", "s86", "s87",
                 "s88", "s89", "s90", "s91", "s92", "s93", "s94", "s95", "s96", "s97", "s98", "s99");
  const int b = blockIdx.x, m = 32 * (b & 7) + (b >> 3);
  const int w = __builtin_amdgcn_readfirstlane(threadIdx.x >> 6);
  if (w < 4) {
    svc_role(lds, m, w, lane_id(), pc);
    __builtin_amdgcn_endpgm();
  }
  attn_role(lds, m, w - 4, lane_id());
}

extern "C" void kernel_launch(void* const* d_in, const int* in_sizes, int n_in, void* d_out, int out_size,
                              void* d_ws, size_t ws_size, hipStream_t stream) {
  Params p;
  p.x = (const u16*)d_in[0]; p.wq = (const u16*)d_in[1]; p.wk = (const u16*)d_in[2]; p.wv = (const u16*)d_in[3];
  p.wo = (const u16*)d_in[4]; p.logA = (const u16*)d_in[6]; p.Aim = (const u16*)d_in[7]; p.Cre = (const u16*)d_in[8];
  p.Cim = (const u16*)d_in[9]; p.logdt = (const u16*)d_in[10]; p.Dskip = (const u16*)d_in[11]; p.gatew = (const u16*)d_in[12];
  p.out = (u16*)d_out; p.ws = (char*)d_ws;
  k_fused<<<256, 768, 0, stream>>>(p);
}
```
